```python
import torch

def _softmax(x, axis=-1):
    return torch.softmax(x, dim=axis)
def _gelu(x):
    return torch.nn.functional.gelu(x, approximate="tanh")
def _silu(x):
    return torch.nn.functional.silu(x)
def _sigmoid(x):
    return torch.sigmoid(x)
def _layernorm(x, axis=-1, eps=1e-5):
    m = x.mean(dim=axis, keepdim=True)
    v = x.var(dim=axis, keepdim=True, unbiased=False)
    return (x - m) / torch.sqrt(v + eps)
def _scatter_add(tgt, idx, src, axis=0):
    return tgt.clone().index_add_(axis, idx, src)
def _segment_sum(src, seg, n):
    out = torch.zeros((n,) + src.shape[1:], dtype=src.dtype)
    return out.index_add_(0, seg, src)
def _pad(x, pw):
    flat = tuple(v for pair in reversed(pw) for v in pair)
    return torch.nn.functional.pad(x, flat)

kernel_name = 'concat_heads__mha_block_cached__compressed_kv_cla__decode'

def setup_inputs():

    _w = (lambda a: (lambda t: t.to(torch.bfloat16) if t.is_floating_point() else t)(torch.as_tensor(a)))
    def _A_setup():
        B, Sq, D = 2, 1, 448
        H, Sk, Dh = 16, 4096, 256
        return {
            "x": _w((lambda *s: torch.randn(*s, dtype=torch.bfloat16) * 1.6)(B, Sq, D)),
            "k_cache": _w((lambda *s: torch.randn(*s, dtype=torch.bfloat16) * 1.6)(B, H, Sk, Dh)),
            "v_cache": _w((lambda *s: torch.randn(*s, dtype=torch.bfloat16) * 0.05)(B, H, Sk, Dh)),
            "wq": _w((lambda *s: torch.randn(*s, dtype=torch.bfloat16) * 1.6)(D, H * Dh) * (D ** -0.5)),
            "wo": _w((lambda *s: torch.randn(*s, dtype=torch.bfloat16) * 0.05)(H * Dh, D) * ((H * Dh) ** -0.5)),
        }
    _A_in = _A_setup()

    def _B_setup():
        B, H, Sq, Sk, D, M = 2, 16, 1, 4096, 256, 448
        Dc = 4 * D
        return {
            "x": _w((lambda *s: torch.randn(*s, dtype=torch.bfloat16) * 3.2)(B, Sq, M)),
            "c_kv": _w((lambda *s: torch.randn(*s, dtype=torch.bfloat16) * 0.1)(B, Sk, Dc)),
            "w_q": _w((lambda *s: torch.randn(*s, dtype=torch.bfloat16) * 6.4)(M, H, D) * (M ** -0.5)),
            "w_uk": _w((lambda *s: torch.randn(*s, dtype=torch.bfloat16) * 6.4)(Dc, H, D) * (Dc ** -0.5)),
            "w_uv": _w((lambda *s: torch.randn(*s, dtype=torch.bfloat16) * 0.1)(Dc, H, D) * (Dc ** -0.5)),
            "w_o": _w((lambda *s: torch.randn(*s, dtype=torch.bfloat16) * 0.1)(H, D, M) * ((H * D) ** -0.5)),
        }
    _B_in = _B_setup()

    return {
        "A_x": _A_in["x"],
        "A_k_cache": _A_in["k_cache"],
        "A_v_cache": _A_in["v_cache"],
        "A_wq": _A_in["wq"],
        "A_wo": _A_in["wo"],
        "B_x": _B_in["x"],
        "B_c_kv": _B_in["c_kv"],
        "B_w_q": _B_in["w_q"],
        "B_w_uk": _B_in["w_uk"],
        "B_w_uv": _B_in["w_uv"],
        "B_w_o": _B_in["w_o"],
    }

def reference(A_x, A_k_cache, A_v_cache, A_wq, A_wo, B_x, B_c_kv, B_w_q, B_w_uk, B_w_uv, B_w_o):
    def _A_ref(x, k_cache, v_cache, wq, wo):
        B, Sq, D = x.shape
        H, Dh = 16, 256
        q = (lambda x, *d: x.permute(*d))((x @ wq).reshape(B, Sq, H, Dh), 0, 2, 1, 3)
        scores = torch.einsum("bhqd,bhkd->bhqk", q, k_cache) * (Dh ** -0.5)
        p = _softmax(scores, axis=-1)
        o = torch.einsum("bhqk,bhkd->bhqd", p, v_cache)
        return (lambda x, *d: x.permute(*d))(o, 0, 2, 1, 3).reshape(B, Sq, H * Dh) @ wo

    def _B_ref(x, c_kv, w_q, w_uk, w_uv, w_o):
        B, Sq, M = x.shape[0], x.shape[1], x.shape[2]
        Sk = c_kv.shape[1]
        H, D = w_q.shape[1], w_q.shape[2]
        q = torch.einsum("bqm,mhd->bhqd", x, w_q)
        k = torch.einsum("bkc,chd->bhkd", c_kv, w_uk)
        v = torch.einsum("bkc,chd->bhkd", c_kv, w_uv)
        scores = torch.einsum("bhqd,bhkd->bhqk", q, k) * (D ** -0.5)
        off = Sk - Sq
        qi = torch.arange(Sq)[:, None]
        kj = torch.arange(Sk)[None, :]
        cmask = kj <= (qi + off)
        scores = torch.where(cmask[None, None], scores, -1e9)
        p = _softmax(scores, axis=-1)
        ctx = torch.einsum("bhqk,bhkd->bhqd", p, v)
        return torch.einsum("bhqd,hdm->bqm", ctx, w_o)

    a = _A_ref(A_x, A_k_cache, A_v_cache, A_wq, A_wo)
    b = _B_ref(B_x, B_c_kv, B_w_q, B_w_uk, B_w_uv, B_w_o)
    return torch.concatenate([a, b], axis=-1)
```

```cpp
#include <hip/hip_runtime.h>
#include <hip/hip_bf16.h>
#include <stdint.h>
#define ROUND_S 0
#define ROUND_P 0
typedef unsigned short u16;
__device__ __forceinline__ float bf2f(u16 v){ return __uint_as_float(((unsigned)v)<<16); }
__device__ __forceinline__ float rbf(float f){ return __bfloat162float(__float2bfloat16(f)); }
__device__ __forceinline__ float wsum(float v){ for(int o=32;o>0;o>>=1) v+=__shfl_xor(v,o); return v; }

__global__ void k_qproj(const u16* xA,const u16* wqA,const u16* xB,const u16* wqB,float* q){
  int which=blockIdx.y; const u16* x=which?xB:xA; const u16* w=which?wqB:wqA;
  int col=blockIdx.x*256+threadIdx.x; float a0=0,a1=0;
  for(int m=0;m<448;m++){ float wv=bf2f(w[(size_t)m*4096+col]); a0+=bf2f(x[m])*wv; a1+=bf2f(x[448+m])*wv; }
  q[which*8192+col]=rbf(a0); q[which*8192+4096+col]=rbf(a1);
}
__global__ void k_scoresA(const u16* K,const float* q,float* s){
  int bh=blockIdx.y; int key=blockIdx.x*4+(threadIdx.x>>6); int lane=threadIdx.x&63;
  const u16* kr=K+((size_t)bh*4096+key)*256+lane*4; const float* qq=q+bh*256+lane*4;
  float a=0; for(int j=0;j<4;j++) a+=bf2f(kr[j])*qq[j];
  a=wsum(a); if(lane==0){ float v=ROUND_S?rbf(a):a; s[bh*4096+key]=v*0.0625f; }
}
__global__ void k_qabs(const u16* wuk,const float* qB,float* qabs){
  int row=blockIdx.x*4+(threadIdx.x>>6); int lane=threadIdx.x&63; int c=row>>4,h=row&15;
  const u16* r=wuk+(size_t)row*256+lane*4; float a0=0,a1=0;
  for(int j=0;j<4;j++){ float w=bf2f(r[j]); a0+=w*qB[h*256+lane*4+j]; a1+=w*qB[4096+h*256+lane*4+j]; }
  a0=wsum(a0); a1=wsum(a1); if(lane==0){ qabs[(0*16+h)*1024+c]=a0; qabs[(16+h)*1024+c]=a1; }
}
__global__ void k_scoresB(const u16* ckv,const float* qabs,float* s){
  int b=blockIdx.y; int key=blockIdx.x*4+(threadIdx.x>>6); int lane=threadIdx.x&63;
  const u16* r=ckv+((size_t)b*4096+key)*1024; float cv[16];
  for(int j=0;j<16;j++) cv[j]=bf2f(r[j*64+lane]);
  for(int h=0;h<16;h++){ float a=0; for(int j=0;j<16;j++) a+=cv[j]*qabs[(b*16+h)*1024+j*64+lane]; a=wsum(a);
    if(lane==0){ float v=ROUND_S?rbf(a):a; s[(b*16+h)*4096+key]=v*0.0625f; } }
}
__global__ void k_softmax(float* s){
  __shared__ float red[4]; float* r=s+(size_t)blockIdx.x*4096; int t=threadIdx.x; float m=-INFINITY;
  for(int i=t;i<4096;i+=256) m=fmaxf(m,r[i]);
  for(int o=32;o>0;o>>=1) m=fmaxf(m,__shfl_xor(m,o));
  if((t&63)==0) red[t>>6]=m; __syncthreads(); m=fmaxf(fmaxf(red[0],red[1]),fmaxf(red[2],red[3])); __syncthreads();
  float l=0; for(int i=t;i<4096;i+=256) l+=__expf(r[i]-m); l=wsum(l);
  if((t&63)==0) red[t>>6]=l; __syncthreads(); l=red[0]+red[1]+red[2]+red[3];
  for(int i=t;i<4096;i+=256){ float p=__expf(r[i]-m)/l; r[i]=ROUND_P?rbf(p):p; }
}
__global__ void k_pvA(const u16* V,const float* p,float* op){
  int bh=blockIdx.y, sp=blockIdx.x; int d=threadIdx.x; float a=0;
  for(int k=sp*256;k<sp*256+256;k++) a+=p[bh*4096+k]*bf2f(V[((size_t)bh*4096+k)*256+d]);
  op[(bh*16+sp)*256+d]=a;
}
__global__ void k_pvB(const u16* ckv,const float* p,float* cp){
  int b=blockIdx.z, sp=blockIdx.y; int c=blockIdx.x*256+threadIdx.x; float a[16]; for(int h=0;h<16;h++) a[h]=0;
  for(int k=sp*128;k<sp*128+128;k++){ float v=bf2f(ckv[((size_t)b*4096+k)*1024+c]); for(int h=0;h<16;h++) a[h]+=p[(b*16+h)*4096+k]*v; }
  for(int h=0;h<16;h++) cp[((b*32+sp)*16+h)*1024+c]=a[h];
}
__global__ void k_mid(const float* op,const float* cp,const u16* wuv,float* o){
  int bh=blockIdx.x; int d=threadIdx.x;
  if(blockIdx.y==0){ float a=0; for(int s=0;s<16;s++) a+=op[(bh*16+s)*256+d]; o[bh*256+d]=rbf(a); }
  else { __shared__ float cl[1024]; int b=bh>>4,h=bh&15;
    for(int c=d;c<1024;c+=256){ float a=0; for(int s=0;s<32;s++) a+=cp[((b*32+s)*16+h)*1024+c]; cl[c]=a; } __syncthreads();
    float a=0; for(int c=0;c<1024;c++) a+=cl[c]*bf2f(wuv[((size_t)c*16+h)*256+d]); o[8192+bh*256+d]=rbf(a); }
}
__global__ void k_out(const float* o,const u16* woA,const u16* woB,__hip_bfloat16* out){
  int idx=blockIdx.x*4+(threadIdx.x>>6); int lane=threadIdx.x&63; int which=idx/896; int r=idx%896; int b=r/448,m=r%448;
  const u16* w=which?woB:woA; float a=0;
  for(int i=lane;i<4096;i+=64) a+=o[which*8192+b*4096+i]*bf2f(w[(size_t)i*448+m]);
  a=wsum(a); if(lane==0) out[b*896+which*448+m]=__float2bfloat16(a);
}
extern "C" void kernel_launch(void* const* d_in,const int* in_sizes,int n_in,void* d_out,int out_size,void* d_ws,size_t ws_size,hipStream_t stream){
  const u16* xA=(const u16*)d_in[0]; const u16* K=(const u16*)d_in[1]; const u16* V=(const u16*)d_in[2]; const u16* wqA=(const u16*)d_in[3]; const u16* woA=(const u16*)d_in[4];
  const u16* xB=(const u16*)d_in[5]; const u16* ckv=(const u16*)d_in[6]; const u16* wqB=(const u16*)d_in[7]; const u16* wuk=(const u16*)d_in[8]; const u16* wuv=(const u16*)d_in[9]; const u16* woB=(const u16*)d_in[10];
  float* ws=(float*)d_ws; float* q=ws; float* s=q+16384; float* qabs=s+64*4096; float* op=qabs+32768; float* cp=op+32*16*256; float* o=cp+2*32*16*1024;
  k_qproj<<<dim3(16,2),256,0,stream>>>(xA,wqA,xB,wqB,q);
  k_scoresA<<<dim3(1024,32),256,0,stream>>>(K,q,s);
  k_qabs<<<4096,256,0,stream>>>(wuk,q+8192,qabs);
  k_scoresB<<<dim3(1024,2),256,0,stream>>>(ckv,qabs,s+32*4096);
  k_softmax<<<64,256,0,stream>>>(s);
  k_pvA<<<dim3(16,32),256,0,stream>>>(V,s,op);
  k_pvB<<<dim3(4,32,2),256,0,stream>>>(ckv,s+32*4096,cp);
  k_mid<<<dim3(32,2),256,0,stream>>>(op,cp,wuv,o);
  k_out<<<448,256,0,stream>>>(o,woA,woB,(__hip_bfloat16*)d_out);
}
```

```cpp
#include <hip/hip_runtime.h>
#include <hip/hip_bf16.h>
#include <stdint.h>
#define ROUND_S 1
#define ROUND_P 1
typedef unsigned short u16;
__device__ __forceinline__ float bf2f(u16 v){ return __uint_as_float(((unsigned)v)<<16); }
__device__ __forceinline__ float rbf(float f){ return __bfloat162float(__float2bfloat16(f)); }
__device__ __forceinline__ float wsum(float v){ for(int o=32;o>0;o>>=1) v+=__shfl_xor(v,o); return v; }

__global__ void k_qproj(const u16* xA,const u16* wqA,const u16* xB,const u16* wqB,float* q){
  int which=blockIdx.y; const u16* x=which?xB:xA; const u16* w=which?wqB:wqA;
  int col=blockIdx.x*256+threadIdx.x; float a0=0,a1=0;
  for(int m=0;m<448;m++){ float wv=bf2f(w[(size_t)m*4096+col]); a0+=bf2f(x[m])*wv; a1+=bf2f(x[448+m])*wv; }
  q[which*8192+col]=rbf(a0); q[which*8192+4096+col]=rbf(a1);
}
__global__ void k_scoresA(const u16* K,const float* q,float* s){
  int bh=blockIdx.y; int key=blockIdx.x*4+(threadIdx.x>>6); int lane=threadIdx.x&63;
  const u16* kr=K+((size_t)bh*4096+key)*256+lane*4; const float* qq=q+bh*256+lane*4;
  float a=0; for(int j=0;j<4;j++) a+=bf2f(kr[j])*qq[j];
  a=wsum(a); if(lane==0){ float v=ROUND_S?rbf(a):a; s[bh*4096+key]=v*0.0625f; }
}

typedef unsigned v4u __attribute__((ext_vector_type(4)));
typedef __bf16 bf16x2 __attribute__((ext_vector_type(2)));
#define USE_NT 1
__device__ __forceinline__ v4u ldg(const v4u* p){ if(USE_NT) return __builtin_nontemporal_load(p); else return *p; }
__device__ __forceinline__ float dot2(unsigned k, unsigned q, float acc){ return __builtin_amdgcn_fdot2_f32_bf16(__builtin_bit_cast(bf16x2,k),__builtin_bit_cast(bf16x2,q),acc,false); }
__global__ void __launch_bounds__(256) k_scoresA2(const u16* K,const float* q,float* s){
  int bh=blockIdx.x>>3, sp=blockIdx.x&7; int tid=threadIdx.x, lane=tid&63, w=tid>>6;
  unsigned qp[4]; for(int j=0;j<4;j++){ float a=q[bh*256+(lane&31)*8+2*j], b=q[bh*256+(lane&31)*8+2*j+1]; qp[j]=(__float_as_uint(a)>>16)|(__float_as_uint(b)&0xffff0000u); }
  const v4u* p=(const v4u*)(K+((size_t)bh*4096+sp*512)*256)+tid;
  v4u buf[16];
  #pragma unroll
  for(int i=0;i<16;i++) buf[i]=ldg(p+i*256);
  for(int it=0;it<4;it++){
    #pragma unroll
    for(int j=0;j<16;j++){
      v4u c=buf[j]; if(it<3) buf[j]=ldg(p+((it+1)*16+j)*256);
      float a=dot2(c.x,qp[0],0.f); a=dot2(c.y,qp[1],a); a=dot2(c.z,qp[2],a); a=dot2(c.w,qp[3],a);
      for(int o=16;o>0;o>>=1) a+=__shfl_xor(a,o);
      if((lane&31)==0){ int key=sp*512+(it*16+j)*8+w*2+(lane>>5); s[bh*4096+key]=rbf(a)*0.0625f; }
    }
  }
}
__global__ void __launch_bounds__(256) k_pvA2(const u16* V,const float* p,float* op){
  __shared__ float ps[2048]; __shared__ float red[32][64];
  int bh=blockIdx.x>>3, dq=(blockIdx.x>>1)&3, kh=blockIdx.x&1; int tid=threadIdx.x;
  const v4u* g=(const v4u*)(V+((size_t)bh*4096+kh*2048+(tid>>3))*256+dq*64)+(tid&7);
  v4u buf[16];
  #pragma unroll
  for(int i=0;i<16;i++) buf[i]=ldg(g+(size_t)i*32*32);
  for(int i=tid;i<2048;i+=256) ps[i]=p[bh*4096+kh*2048+i];
  __syncthreads();
  float acc[8]; for(int j=0;j<8;j++) acc[j]=0;
  for(int it=0;it<4;it++){
    #pragma unroll
    for(int j=0;j<16;j++){
      v4u c=buf[j]; if(it<3) buf[j]=ldg(g+(size_t)((it+1)*16+j)*32*32);
      float pp=ps[(it*16+j)*32+(tid>>3)];
      #pragma unroll
      for(int e=0;e<4;e++){ acc[2*e]+=pp*__uint_as_float(c[e]<<16); acc[2*e+1]+=pp*__uint_as_float(c[e]&0xffff0000u); }
    }
  }
  for(int j=0;j<8;j++) red[tid>>3][(tid&7)*8+j]=acc[j];
  __syncthreads();
  if(tid<64){ float a=0; for(int r=0;r<32;r++) a+=red[r][tid];
    op[(bh*16+kh)*256+dq*64+tid]=a; for(int sp=2+kh;sp<16;sp+=2) op[(bh*16+sp)*256+dq*64+tid]=0.f; }
}

__global__ void k_qabs(const u16* wuk,const float* qB,float* qabs){
  int row=blockIdx.x*4+(threadIdx.x>>6); int lane=threadIdx.x&63; int c=row>>4,h=row&15;
  const u16* r=wuk+(size_t)row*256+lane*4; float a0=0,a1=0;
  for(int j=0;j<4;j++){ float w=bf2f(r[j]); a0+=w*qB[h*256+lane*4+j]; a1+=w*qB[4096+h*256+lane*4+j]; }
  a0=wsum(a0); a1=wsum(a1); if(lane==0){ qabs[(0*16+h)*1024+c]=a0; qabs[(16+h)*1024+c]=a1; }
}
__global__ void k_scoresB(const u16* ckv,const float* qabs,float* s){
  int b=blockIdx.y; int key=blockIdx.x*4+(threadIdx.x>>6); int lane=threadIdx.x&63;
  const u16* r=ckv+((size_t)b*4096+key)*1024; float cv[16];
  for(int j=0;j<16;j++) cv[j]=bf2f(r[j*64+lane]);
  for(int h=0;h<16;h++){ float a=0; for(int j=0;j<16;j++) a+=cv[j]*qabs[(b*16+h)*1024+j*64+lane]; a=wsum(a);
    if(lane==0){ float v=ROUND_S?rbf(a):a; s[(b*16+h)*4096+key]=v*0.0625f; } }
}
__global__ void k_softmax(float* s){
  __shared__ float red[4]; float* r=s+(size_t)blockIdx.x*4096; int t=threadIdx.x; float m=-INFINITY;
  for(int i=t;i<4096;i+=256) m=fmaxf(m,r[i]);
  for(int o=32;o>0;o>>=1) m=fmaxf(m,__shfl_xor(m,o));
  if((t&63)==0) red[t>>6]=m; __syncthreads(); m=fmaxf(fmaxf(red[0],red[1]),fmaxf(red[2],red[3])); __syncthreads();
  float l=0; for(int i=t;i<4096;i+=256) l+=__expf(r[i]-m); l=wsum(l);
  if((t&63)==0) red[t>>6]=l; __syncthreads(); l=red[0]+red[1]+red[2]+red[3];
  for(int i=t;i<4096;i+=256){ float p=__expf(r[i]-m)/l; r[i]=ROUND_P?rbf(p):p; }
}
__global__ void k_pvA(const u16* V,const float* p,float* op){
  int bh=blockIdx.y, sp=blockIdx.x; int d=threadIdx.x; float a=0;
  for(int k=sp*256;k<sp*256+256;k++) a+=p[bh*4096+k]*bf2f(V[((size_t)bh*4096+k)*256+d]);
  op[(bh*16+sp)*256+d]=a;
}
__global__ void k_pvB(const u16* ckv,const float* p,float* cp){
  int b=blockIdx.z, sp=blockIdx.y; int c=blockIdx.x*256+threadIdx.x; float a[16]; for(int h=0;h<16;h++) a[h]=0;
  for(int k=sp*128;k<sp*128+128;k++){ float v=bf2f(ckv[((size_t)b*4096+k)*1024+c]); for(int h=0;h<16;h++) a[h]+=p[(b*16+h)*4096+k]*v; }
  for(int h=0;h<16;h++) cp[((b*32+sp)*16+h)*1024+c]=a[h];
}
__global__ void k_mid(const float* op,const float* cp,const u16* wuv,float* o){
  int bh=blockIdx.x; int d=threadIdx.x;
  if(blockIdx.y==0){ float a=0; for(int s=0;s<16;s++) a+=op[(bh*16+s)*256+d]; o[bh*256+d]=rbf(a); }
  else { __shared__ float cl[1024]; int b=bh>>4,h=bh&15;
    for(int c=d;c<1024;c+=256){ float a=0; for(int s=0;s<32;s++) a+=cp[((b*32+s)*16+h)*1024+c]; cl[c]=a; } __syncthreads();
    float a=0; for(int c=0;c<1024;c++) a+=cl[c]*bf2f(wuv[((size_t)c*16+h)*256+d]); o[8192+bh*256+d]=rbf(a); }
}
__global__ void k_out(const float* o,const u16* woA,const u16* woB,__hip_bfloat16* out){
  int idx=blockIdx.x*4+(threadIdx.x>>6); int lane=threadIdx.x&63; int which=idx/896; int r=idx%896; int b=r/448,m=r%448;
  const u16* w=which?woB:woA; float a=0;
  for(int i=lane;i<4096;i+=64) a+=o[which*8192+b*4096+i]*bf2f(w[(size_t)i*448+m]);
  a=wsum(a); if(lane==0) out[b*896+which*448+m]=__float2bfloat16(a);
}
extern "C" void kernel_launch(void* const* d_in,const int* in_sizes,int n_in,void* d_out,int out_size,void* d_ws,size_t ws_size,hipStream_t stream){
  const u16* xA=(const u16*)d_in[0]; const u16* K=(const u16*)d_in[1]; const u16* V=(const u16*)d_in[2]; const u16* wqA=(const u16*)d_in[3]; const u16* woA=(const u16*)d_in[4];
  const u16* xB=(const u16*)d_in[5]; const u16* ckv=(const u16*)d_in[6]; const u16* wqB=(const u16*)d_in[7]; const u16* wuk=(const u16*)d_in[8]; const u16* wuv=(const u16*)d_in[9]; const u16* woB=(const u16*)d_in[10];
  float* ws=(float*)d_ws; float* q=ws; float* s=q+16384; float* qabs=s+64*4096; float* op=qabs+32768; float* cp=op+32*16*256; float* o=cp+2*32*16*1024;
  k_qproj<<<dim3(16,2),256,0,stream>>>(xA,wqA,xB,wqB,q);
  k_scoresA2<<<256,256,0,stream>>>(K,q,s);
  k_qabs<<<4096,256,0,stream>>>(wuk,q+8192,qabs);
  k_scoresB<<<dim3(1024,2),256,0,stream>>>(ckv,qabs,s+32*4096);
  k_softmax<<<64,256,0,stream>>>(s);
  k_pvA2<<<256,256,0,stream>>>(V,s,op);
  k_pvB<<<dim3(4,32,2),256,0,stream>>>(ckv,s+32*4096,cp);
  k_mid<<<dim3(32,2),256,0,stream>>>(op,cp,wuv,o);
  k_out<<<448,256,0,stream>>>(o,woA,woB,(__hip_bfloat16*)d_out);
}
```

```cpp
#include <hip/hip_runtime.h>
#include <stdint.h>

typedef unsigned u32;
typedef unsigned short u16;
typedef u32 v4u __attribute__((ext_vector_type(4)));
typedef u32 v2u __attribute__((ext_vector_type(2)));
typedef float f32x4 __attribute__((ext_vector_type(4)));
typedef float f32x2 __attribute__((ext_vector_type(2)));
typedef __bf16 bf16x2 __attribute__((ext_vector_type(2)));
typedef __bf16 bf16x8 __attribute__((ext_vector_type(8)));
typedef __amdgpu_buffer_rsrc_t rsrc_t;
#define LAS __attribute__((address_space(3)))

constexpr int WS_EPOCH = 0;
constexpr int WS_QA = 4096;
constexpr int WS_QB = WS_QA + 131072;
constexpr int WS_SA = WS_QB + 131072;
constexpr int WS_YA = WS_SA + 524288;
constexpr int WS_YB = WS_YA + 524288;
constexpr int WS_QABS = WS_YB + 524288;
constexpr int WS_PB = WS_QABS + 131072;
constexpr int WS_PBS = WS_PB + 524288;
constexpr int WS_CP = WS_PBS + 131072;
constexpr int WS_CPS = WS_CP + 2097152;
constexpr int WS_XP = WS_CPS + 8192;
constexpr int WS_END = WS_XP + 524288;

constexpr int L_CTL = 0;
constexpr int L_Q = 16;
constexpr int L_PBUF = L_Q + 384;
constexpr int L_ORED = L_PBUF;
constexpr int L_OB = L_PBUF + 1024;
constexpr int L_S3S = L_OB + 64;
constexpr int L_YB = L_S3S + 256;
constexpr int L_STAMP = L_YB + 16;
constexpr int L_S3A = L_STAMP + 256;
constexpr int L_QRED = L_S3A;
constexpr int L_XR = L_S3A;
constexpr int L_CW = L_S3A + 2048;
constexpr int L_S5 = L_CW + 512;
constexpr int L_T0 = L_S3A + 8 * 16 * 36;
constexpr int L_S3B = L_T0;
constexpr int L_T1 = L_T0 + 16384;
constexpr int L_TOTAL = L_T1 + 16384;
static_assert(L_TOTAL * 4 > 81920 && L_TOTAL * 4 <= 160 * 1024, "lds");

#define TPOLL 4
#define POLLS 10
constexpr int AUX_NT = 2, AUX_SC1 = 16;
constexpr float LOG2E = 1.4426950408889634f;
constexpr u32 SPIN_CAP = 1u << 17;

__device__ __forceinline__ rsrc_t mkrs(const void* p, u32 bytes) { return __builtin_amdgcn_make_buffer_rsrc((void*)p, 0, bytes, 0x00020000); }
template <int AUX> __device__ __forceinline__ v4u ld16(rsrc_t r, u32 voff, u32 soff) { return __builtin_amdgcn_raw_buffer_load_b128(r, voff, soff, AUX); }
__device__ __forceinline__ void st8(rsrc_t r, u32 off, u32 data, u32 tag) { __builtin_amdgcn_raw_buffer_store_b64(v2u{data, tag}, r, off, 0, AUX_SC1); }
__device__ __forceinline__ void st16(rsrc_t r, u32 off, u32 d0, u32 d1, u32 tag) { __builtin_amdgcn_raw_buffer_store_b128(v4u{d0, tag, d1, tag}, r, off, 0, AUX_SC1); }
__device__ __forceinline__ float asf(u32 x) { return __builtin_bit_cast(float, x); }
__device__ __forceinline__ u32 asu(float x) { return __builtin_bit_cast(u32, x); }
__device__ __forceinline__ float bflo(u32 x) { return asf(x << 16); }
__device__ __forceinline__ float bfhi(u32 x) { return asf(x & 0xffff0000u); }
__device__ __forceinline__ float dot2(u32 a, u32 b, float acc) {
  return __builtin_amdgcn_fdot2_f32_bf16(__builtin_bit_cast(bf16x2, a), __builtin_bit_cast(bf16x2, b), acc, false);
}
__device__ __forceinline__ u32 cvtpk(float lo, float hi) {
  bf16x2 r; r[0] = (__bf16)lo; r[1] = (__bf16)hi; return __builtin_bit_cast(u32, r);
}
__device__ __forceinline__ float ex2(float x) { return __builtin_amdgcn_exp2f(x); }
template <int CTRL> __device__ __forceinline__ float dpp(float x) {
  return asf((u32)__builtin_amdgcn_mov_dpp((int)asu(x), CTRL, 0xf, 0xf, true));
}
constexpr int XOR1 = 0xB1, XOR2 = 0x4E, XOR7 = 0x141, XOR8 = 0x128;
__device__ __forceinline__ float tr16(float a, float b) {
  auto s = __builtin_amdgcn_permlane16_swap(asu(a), asu(b), false, false); return asf(s[0]) + asf(s[1]);
}
__device__ __forceinline__ float tr32(float a, float b) {
  auto s = __builtin_amdgcn_permlane32_swap(asu(a), asu(b), false, false); return asf(s[0]) + asf(s[1]);
}
__device__ __forceinline__ float mx16(float a) { auto s = __builtin_amdgcn_permlane16_swap(asu(a), asu(a), false, false); return fmaxf(asf(s[0]), asf(s[1])); }
__device__ __forceinline__ float mx32(float a) { auto s = __builtin_amdgcn_permlane32_swap(asu(a), asu(a), false, false); return fmaxf(asf(s[0]), asf(s[1])); }
template <int CTRL> __device__ __forceinline__ float trd(float lo, float hi, bool bit) {
  const float keep = bit ? hi : lo, send = bit ? lo : hi; return keep + dpp<CTRL>(send);
}
__device__ __forceinline__ float rowsum16(float x) { x += dpp<XOR1>(x); x += dpp<XOR2>(x); x += dpp<XOR7>(x); x += dpp<XOR8>(x); return x; }
__device__ __forceinline__ float rowmax16(float x) { x = fmaxf(x, dpp<XOR1>(x)); x = fmaxf(x, dpp<XOR2>(x)); x = fmaxf(x, dpp<XOR7>(x)); x = fmaxf(x, dpp<XOR8>(x)); return x; }
__device__ __forceinline__ float red16x32(const float (&x)[16], int lane) {
  float y[8], z[4], u[2];
#pragma unroll
  for (int i = 0; i < 8; ++i) y[i] = tr16(x[i], x[i + 8]);
#pragma unroll
  for (int i = 0; i < 4; ++i) z[i] = trd<XOR8>(y[i], y[i + 4], lane & 8);
#pragma unroll
  for (int i = 0; i < 2; ++i) u[i] = trd<XOR7>(z[i], z[i + 2], lane & 4);
  float v = trd<XOR2>(u[0], u[1], lane & 2);
  return v + dpp<XOR1>(v);
}
__device__ __forceinline__ bool tags_ok(v4u g, u32 tag) { return g.y == tag && g.w == tag; }
__device__ __forceinline__ bool all_ok(bool ok) { return __builtin_amdgcn_ballot_w64(!ok) == 0ull; }
#define CBAR() asm volatile("" ::: "memory")
#define SBAR() __builtin_amdgcn_sched_barrier(0)
#define LGKM0() asm volatile("s_waitcnt lgkmcnt(0)" ::: "memory")

__device__ __forceinline__ void gbar(LAS u32* ctr, u32& phase, int lane) {
  phase += 4;
  LGKM0();
  if (lane == 0) __hip_atomic_fetch_add(ctr, 1u, __ATOMIC_RELAXED, __HIP_MEMORY_SCOPE_WORKGROUP);
#pragma unroll 1
  for (u32 s = 0; s < (1u << 24); ++s) {
    const u32 v = __builtin_amdgcn_readfirstlane(__hip_atomic_load(ctr, __ATOMIC_RELAXED, __HIP_MEMORY_SCOPE_WORKGROUP));
    if (v >= phase) break;
  }
  CBAR();
}
template <int N, int STRIDE> __device__ __forceinline__ void gather_wait(v4u (&g)[N], rsrc_t r, u32 voff, u32 soff, u32 tag) {
  u32 pend = 0;
#pragma unroll
  for (int i = 0; i < N; ++i) if (!all_ok(tags_ok(g[i], tag))) pend |= 1u << i;
  if (pend) {
    u32 s = 0;
    if (N > 4) {
      const u32 so = soff + (u32)__builtin_ctz(pend) * STRIDE;
#pragma unroll 1
      for (; s < SPIN_CAP; ++s) {
        __builtin_amdgcn_s_sleep(POLLS);
        asm volatile("" : "+v"(voff) :: "memory");
        const v4u c = ld16<AUX_SC1>(r, voff, so);
        if (all_ok(tags_ok(c, tag))) break;
      }
    }
    bool ok = false;
    if (N <= 4) __builtin_amdgcn_s_sleep(POLLS);
#pragma unroll 1
    for (; s < SPIN_CAP; ++s) {
      asm volatile("" : "+v"(voff) :: "memory");
      if (!ok) {
#pragma unroll
        for (int i = 0; i < N; ++i) g[i] = ld16<AUX_SC1>(r, voff, soff + i * STRIDE);
      }
      ok = true;
#pragma unroll
      for (int i = 0; i < N; ++i) ok = ok && tags_ok(g[i], tag);
      if (all_ok(ok)) break;
      __builtin_amdgcn_s_sleep(POLLS);
    }
  }
}
template <int N, int STRIDE> __device__ __forceinline__ void gather_wait_slow(v4u (&g)[N], rsrc_t r, u32 voff, u32 soff, u32 tag) {
#pragma unroll 1
  for (u32 s = 0; s < SPIN_CAP; ++s) {
    bool ok = true;
#pragma unroll
    for (int i = 0; i < N; ++i) ok = ok && tags_ok(g[i], tag);
    if (all_ok(ok)) break;
    __builtin_amdgcn_s_sleep(32);
    asm volatile("" : "+v"(voff) :: "memory");
    if (!ok) {
#pragma unroll
      for (int i = 0; i < N; ++i) g[i] = ld16<AUX_SC1>(r, voff, soff + i * STRIDE);
    }
  }
}
template <int N, int SL = POLLS, class F> __device__ __forceinline__ void gwait(v4u (&g)[N], u32 tag, F&& ld) {
#pragma unroll 1
  for (u32 s = 0; s < SPIN_CAP; ++s) {
    bool ok = true;
#pragma unroll
    for (int i = 0; i < N; ++i) ok = ok && tags_ok(g[i], tag);
    if (all_ok(ok)) break;
    __builtin_amdgcn_s_sleep(SL);
    CBAR();
    if (!ok) {
#pragma unroll
      for (int i = 0; i < N; ++i) g[i] = ld(i);
    }
  }
}
template <int N, class F> __device__ __forceinline__ void gissue(v4u (&g)[N], F&& ld) {
#pragma unroll
  for (int i = 0; i < N; ++i) g[i] = ld(i);
}
__device__ __forceinline__ void slot(unsigned long long t0, u32 ticks) {
#pragma unroll 1
  while ((u32)(__builtin_amdgcn_s_memrealtime() - t0) < ticks) __builtin_amdgcn_s_sleep(2);
}
template <int N, int STRIDE> __device__ __forceinline__ void gather_issue(v4u (&g)[N], rsrc_t r, u32 voff, u32 soff) {
#pragma unroll
  for (int i = 0; i < N; ++i) g[i] = ld16<AUX_SC1>(r, voff, soff + i * STRIDE);
}
__device__ __forceinline__ void lds_wait(LAS u32* flag, u32 want) {
#pragma unroll 1
  for (u32 s = 0; s < (1u << 24); ++s) {
    if ((u32)__builtin_amdgcn_readfirstlane(__hip_atomic_load(flag, __ATOMIC_RELAXED, __HIP_MEMORY_SCOPE_WORKGROUP)) >= want) break;
    __builtin_amdgcn_s_sleep(1);
  }
  CBAR();
}
#define STAMP(k) do { } while (0)
#define STAMP_INIT() do { } while (0)
#define STAMP_DUMP() do { } while (0)

#define DA 5
#define DPRE 3
#define WB 4
#define TB1 4
#define TB3 8
#define QSLEEP 16
#define TB2 8
#define WO_WIN 4
#define TA_AUX 0
#define TB_AUX 0
#define GPOS 6
#define WT_WIN 6
#define DS 5
#define NPRE 6
#define ASLEEP 32
#define PSLEEP 8
#define CSLEEP 16
#define WO1 16
#define T_Q 460
#define T_A 790
#define T_P 1290
#define T_C 1680
#define T_X 2140
#define WOA 6
#define WOB 12
#define DA1 3
#define NSW 48
#define TB1U 4
#define WV_WIN 3
__host__ __device__ constexpr int adist(int n) { return n < NSW ? DA1 : DA; }
template <int N> __device__ __forceinline__ void vmw() { asm volatile("s_waitcnt vmcnt(%0)" :: "i"(N) : "memory"); }
#define WGBAR() { LGKM0(); __builtin_amdgcn_s_barrier(); CBAR(); }
#define MFMA(A, B, C) __builtin_amdgcn_mfma_f32_16x16x32_bf16(__builtin_bit_cast(bf16x8, A), __builtin_bit_cast(bf16x8, B), C, 0, 0, 0)
#define PSEL(i) (((i) & 1) ? 0x07060302u : 0x05040100u)
#define BFRAG(T, O, i) v4u{__builtin_amdgcn_perm(T[(O) + 1][(i) >> 1], T[(O)][(i) >> 1], PSEL(i)), __builtin_amdgcn_perm(T[(O) + 3][(i) >> 1], T[(O) + 2][(i) >> 1], PSEL(i)), \
                           __builtin_amdgcn_perm(T[(O) + 5][(i) >> 1], T[(O) + 4][(i) >> 1], PSEL(i)), __builtin_amdgcn_perm(T[(O) + 7][(i) >> 1], T[(O) + 6][(i) >> 1], PSEL(i))}
#define SEL4(A0, A1, A2, A3) (g4 == 0 ? (A0) : g4 == 1 ? (A1) : g4 == 2 ? (A2) : (A3))
constexpr f32x4 Z4 = {0.f, 0.f, 0.f, 0.f};

#define QSLICE() { float a[16]; \
    _Pragma("unroll") for (int e = 0; e < 16; ++e) a[e] = 0.f; \
    _Pragma("unroll") for (int i = 0; i < 7; ++i) { \
      asm volatile("" : "+v"(wqv[i])); \
      const float x0 = bflo(xv[0][i]), x1 = bflo(xv[1][i]); \
      _Pragma("unroll") for (int e = 0; e < 4; ++e) { const float lo = bflo(wqv[i][e]), hi = bfhi(wqv[i][e]); \
        a[2 * e] = fmaf(x0, lo, a[2 * e]); a[2 * e + 1] = fmaf(x0, hi, a[2 * e + 1]); \
        a[8 + 2 * e] = fmaf(x1, lo, a[8 + 2 * e]); a[9 + 2 * e] = fmaf(x1, hi, a[9 + 2 * e]); } } \
    float a8[8], a4[4]; \
    _Pragma("unroll") for (int e = 0; e < 8; ++e) a8[e] = tr32(a[e], a[e + 8]);              \
    _Pragma("unroll") for (int e = 0; e < 4; ++e) a4[e] = tr16(a8[e], a8[e + 4]);            \
    const float c0 = trd<XOR8>(a4[0], a4[2], lane & 8), c1 = trd<XOR8>(a4[1], a4[3], lane & 8);     \
    *(LAS f32x2*)(lds + L_QRED + w * 128 + hf * 64 + (lane & 7) * 8 + (g4 & 1) * 4 + ((lane >> 3) & 1) * 2) = f32x2{c0, c1}; \
    gbar(ctl, phase, lane); \
    if (w == 0) { f32x2 s = {0.f, 0.f}; \
      _Pragma("unroll") for (int ww = 0; ww < 4; ++ww) s += *(LAS f32x2*)(lds + L_QRED + ww * 128 + 2 * lane); \
      st16(rW, (isB ? WS_QB : WS_QA) + ((((h * 2 + hf) * 2 + (r8 >> 2)) * 256) + (r8 & 3) * 64 + 2 * rl) * 8, asu(s[0]), asu(s[1]), tag); } }
#define QGATHER(BASE, BB, LQ, FLAG) { v4u g[4]; const u32 qo = lane * 16, qs = (BASE) + (h * 2 + (BB)) * 4096; \
    auto ld = [&](int i) { return ld16<AUX_SC1>(rW, qo, qs + i * 1024); }; gissue<4>(g, ld); gwait<4>(g, tag, ld); \
    ((LAS u32*)(lds + L_Q))[(LQ) + lane] = cvtpk(asf(g[0].x) + asf(g[2].x), asf(g[0].z) + asf(g[2].z)); \
    ((LAS u32*)(lds + L_Q))[(LQ) + 64 + lane] = cvtpk(asf(g[1].x) + asf(g[3].x), asf(g[1].z) + asf(g[3].z)); \
    LGKM0(); \
    if (lane == 0) __hip_atomic_fetch_add(ctl + (FLAG), 1u, __ATOMIC_RELAXED, __HIP_MEMORY_SCOPE_WORKGROUP); }
#define KDOT(s) { float d = dot2(c.x, qp[0], 0.f); d = dot2(c.y, qp[1], d); d = dot2(c.z, qp[2], d); xs[s] = dot2(c.w, qp[3], d); \
    asm volatile("" : "+v"(xs[s])); SBAR(); }
#define PUBLISH_SCORES(SG, SA_, SB_) { const int idx = (lane >> 1) & 15; \
    if (!(lane & 1)) st8(rW, WS_SA + (((bh * 16 + (SG)) * 4 + w) * 32 + (idx & 3) * 8 + hf * 4 + (idx >> 2)) * 8, cvtpk(SA_, SB_), tag); }
__global__ void __launch_bounds__(512, 2)
fused_decode(const u16* __restrict__ xA, const u16* __restrict__ Kc, const u16* __restrict__ Vc, const u16* __restrict__ wqA,
             const u16* __restrict__ woA, const u16* __restrict__ xB, const u16* __restrict__ ckv, const u16* __restrict__ wqB,
             const u16* __restrict__ wuk, const u16* __restrict__ wuv, const u16* __restrict__ woB, u16* out,
             unsigned char* ws) {
  __shared__ __attribute__((aligned(16))) float lds_[L_TOTAL];
  LAS float* lds = (LAS float*)lds_;
  LAS u32* ctl = (LAS u32*)lds_;
  const int tid = threadIdx.x, lane = tid & 63;
  const int wave = __builtin_amdgcn_readfirstlane(tid >> 6), hw = wave >> 2, w = wave & 3;
  const int id = blockIdx.x, x = id & 7, y = id >> 3;
  const int h = x + 8 * (y >> 4), r = y & 15, r8 = r & 7;
  const bool isB = r < 8;
  const int hf = lane >> 5, rl = lane & 31, l15 = lane & 15, g4 = lane >> 4;
  const int vb = r >> 3, dq = r8 & 3, kh = r8 >> 2, bh = vb * 16 + h;
  const int grp = x * 4 + (y >> 3), ab = grp >> 4, kg = grp & 15, mm = r8, kb = 8 * kg + mm;
  const rsrc_t rW = mkrs(ws, WS_END);
  const unsigned long long t0 = __builtin_amdgcn_s_memrealtime();
  STAMP_INIT();
  STAMP(0);
  u32 tag, phase = 0;

  if (hw == 0) {
    const rsrc_t rK = mkrs(Kc, 64u << 20), rV = mkrs(Vc, 64u << 20);
    const u32 kvo = w * 1024 + lane * 16, vvo = (8 * w + (lane >> 3)) * 512 + (lane & 7) * 16;
    const u32 ks0 = (u32)(bh * 4096 + 256 * r8) * 512;
    const u32 vs0 = (u32)(bh * 4096 + 1024 * kh) * 512 + dq * 128;
    v4u buf[16];
    if (w == 0) {
      const u32 ep = __builtin_amdgcn_raw_buffer_load_b32(rW, WS_EPOCH, 0, AUX_SC1);
      if (tid == 0) { ctl[0] = 0; ctl[1] = 0; ctl[2] = ep + 1u; ctl[3] = 0; ctl[4] = 0; ctl[5] = 0; ctl[6] = 0; ctl[8] = 0; ctl[9] = 0; ctl[10] = 0; ctl[11] = 0; }
    }
    WGBAR();
    tag = __builtin_amdgcn_readfirstlane(ctl[2]);
#define ISSUE(m) { if ((m) < 64) buf[(m) & 15] = ld16<AUX_NT>(rK, kvo, ks0 + ((m) >> 5) * (2048 * 512) + ((m) & 31) * 4096); \
      else if ((m) < 128) buf[(m) & 15] = ld16<AUX_NT>(rV, vvo, vs0 + (((m) >> 5) & 1) * (2048 * 512) + ((m) & 31) * 16384); }
    {
      const rsrc_t rC = mkrs(ckv, 2 * 4096 * 1024 * 2);
      const u32 avo = (l15 & 7) * 2048 + w * 512 + (l15 >> 3) * 64 + g4 * 16, as0 = (u32)(ab * 4096 + 32 * kb) * 2048;
      const u32 bvo = (64 * w + 8 * g4) * 2048 + l15 * 16, bs0 = (u32)(ab * 4096 + 256 * kg) * 2048 + r8 * 256;
#define TISSUE(m) { if ((m) < 16) sd[(m) % DS] = ld16<0>(rC, avo + ((m) & 3) * 128, as0 + ((m) >> 2) * 8 * 2048); \
        else if ((m) < 32) sd[(m) % DS] = ld16<0>(rC, bvo, bs0 + ((((m) >> 3) & 1) * 32 + ((m) & 7)) * 2048); }
      v4u sd[DS];
#pragma unroll
      for (int m = 0; m < DS; ++m) TISSUE(m)
      SBAR();
#pragma unroll
      for (int m = 0; m < 32; ++m) {
        const v4u c = sd[m % DS];
        TISSUE(m + DS)
        if (m >= 32 - NPRE) ISSUE(m - (32 - NPRE))
        *(LAS v4u*)(lds + (m < 16 ? L_T0 : L_T1) + ((w * 16 + (m & 15)) * 64 + lane) * 4) = c;
        if (m == 15 || m == 31) { LGKM0(); if (lane == 0) __hip_atomic_store(ctl + 8 + w, (u32)(m >> 4) + 1u, __ATOMIC_RELAXED, __HIP_MEMORY_SCOPE_WORKGROUP); }
        SBAR();
      }
    }
    STAMP(6);
    lds_wait(ctl + 3, 1);
    const v4u qp = *(LAS v4u*)(lds + L_Q + 4 * rl);
    STAMP(1);
#define REFILL(n) { if ((n) < DA1) { ISSUE((n) + NPRE) } else if ((n) >= NPRE) { \
      _Pragma("unroll") for (int m_ = (n) + adist((n) - 1); m_ <= (n) + adist(n); ++m_) { ISSUE(m_) } } }
#define PPOS(i) (4 * (2 * ((i) >> 3) + (((i) >> 1) & 1)) + 2 * ((i) & 1) + (((i) >> 2) & 1))
    const u32 go_s = WS_SA + (((bh * 16 + 4 * kh + ((lane >> 1) & 3)) * 4 + (lane >> 4)) * 32 + w * 8 + ((lane >> 3) & 1) * 4 + 2 * (lane & 1)) * 8;
    v4u gs[1];
    {
      float xs[16], sa, sb;
#pragma unroll
      for (int sl = 0; sl < 2; ++sl) {
#pragma unroll
        for (int s = 0; s < 16; ++s) { const v4u c = buf[s]; REFILL(32 * sl + s) KDOT(s) }
        sa = red16x32(xs, lane);
#pragma unroll
        for (int s = 0; s < 16; ++s) {
          if (sl == 1 && s == 16 - GPOS) { gather_issue<1, 0>(gs, rW, go_s, 0); SBAR(); }
          const v4u c = buf[s]; REFILL(32 * sl + 16 + s) KDOT(s)
        }
        sb = red16x32(xs, lane);
        PUBLISH_SCORES(8 * sl + r8, sa, sb)
        STAMP(2 + sl);
      }
    }
    LAS float* pb = lds + L_PBUF + w * 256;
    float acc[8], pv[32], mrun = -INFINITY;
#pragma unroll
    for (int e = 0; e < 8; ++e) acc[e] = 0.f;
#define MAKE_P() { \
    const float s0 = bflo(gs[0].x) * 0.0625f, s1 = bfhi(gs[0].x) * 0.0625f, s2 = bflo(gs[0].z) * 0.0625f, s3 = bfhi(gs[0].z) * 0.0625f; \
    const float mn = fmaxf(mrun, mx32(mx16(rowmax16(fmaxf(fmaxf(s0, s1), fmaxf(s2, s3)))))); \
    const float sc = ex2((mrun - mn) * LOG2E); mrun = mn; \
    _Pragma("unroll") for (int e = 0; e < 8; ++e) acc[e] *= sc; \
    *(LAS f32x4*)(pb + lane * 4) = f32x4{ex2((s0 - mn) * LOG2E), ex2((s1 - mn) * LOG2E), ex2((s2 - mn) * LOG2E), ex2((s3 - mn) * LOG2E)}; \
    LGKM0(); \
    _Pragma("unroll") for (int u = 0; u < 8; ++u) { const f32x4 t = *(LAS f32x4*)(pb + (lane >> 3) * 32 + u * 4); \
      pv[4 * u] = t[0]; pv[4 * u + 1] = t[1]; pv[4 * u + 2] = t[2]; pv[4 * u + 3] = t[3]; } \
    LGKM0(); }
#define VSTEPS(N0, S0, S1) _Pragma("unroll") for (int s = (S0); s < (S1); ++s) { const v4u c = buf[s & 15]; REFILL((N0) + s) \
      const float p = pv[PPOS(s)]; \
      _Pragma("unroll") for (int e = 0; e < 4; ++e) { acc[2 * e] = fmaf(p, bflo(c[e]), acc[2 * e]); acc[2 * e + 1] = fmaf(p, bfhi(c[e]), acc[2 * e + 1]); } \
      asm volatile("" : "+v"(acc[0]), "+v"(acc[1]), "+v"(acc[2]), "+v"(acc[3]), "+v"(acc[4]), "+v"(acc[5]), "+v"(acc[6]), "+v"(acc[7])); SBAR(); }
    gather_wait<1, 0>(gs, rW, go_s, 0, tag);
    MAKE_P()
    VSTEPS(64, 0, 32 - GPOS)
    gather_issue<1, 0>(gs, rW, go_s, 8192);
    SBAR();
    VSTEPS(64, 32 - GPOS, 32)
    STAMP(4);
    gather_wait<1, 0>(gs, rW, go_s, 8192, tag);
    MAKE_P()
    VSTEPS(96, 0, 32)
    lds_wait(ctl + 6, 4);
    float fin;
    {
      const f32x4 m4 = *(LAS f32x4*)(lds + L_YB), l4 = *(LAS f32x4*)(lds + L_YB + 4);
      const float M = fmaxf(fmaxf(m4[0], m4[1]), fmaxf(m4[2], m4[3]));
      const float L = (l4[0] * ex2((m4[0] - M) * LOG2E) + l4[1] * ex2((m4[1] - M) * LOG2E)) + (l4[2] * ex2((m4[2] - M) * LOG2E) + l4[3] * ex2((m4[3] - M) * LOG2E));
      fin = ex2((mrun - M) * LOG2E) / L;
    }
    float r4[4], r2[2];
#pragma unroll
    for (int e = 0; e < 4; ++e) r4[e] = tr32(acc[e], acc[e + 4]);
#pragma unroll
    for (int e = 0; e < 2; ++e) r2[e] = tr16(r4[e], r4[e + 2]);
    lds[L_ORED + w * 256 + 8 * (lane & 7) + 4 * hf + 2 * (g4 & 1) + ((lane >> 3) & 1)] = trd<XOR8>(r2[0], r2[1], lane & 8) * fin;
    LGKM0();
    u32 old = 0;
    if (lane == 0) old = __hip_atomic_fetch_add(ctl + 1, 1u, __ATOMIC_RELAXED, __HIP_MEMORY_SCOPE_WORKGROUP);
    old = __builtin_amdgcn_readfirstlane(old);
    CBAR();
    if (old == 3) {
      const float o = (lds[L_ORED + lane] + lds[L_ORED + 256 + lane]) + (lds[L_ORED + 512 + lane] + lds[L_ORED + 768 + lane]);
      const u32 hi = cvtpk(o, 0.f);
      ((LAS u16*)(lds + L_OB))[lane] = (u16)hi;
      ((LAS u16*)(lds + L_OB))[64 + lane] = (u16)cvtpk(o - bflo(hi), 0.f);
      LGKM0();
      if (lane == 0) __hip_atomic_store(ctl + 4, 1u, __ATOMIC_RELAXED, __HIP_MEMORY_SCOPE_WORKGROUP);
    }
    STAMP(5);
    STAMP_DUMP();
    return;
  }

  v4u wqv[7]; u32 xv[2][7];
  {
    const rsrc_t rX = mkrs(isB ? xB : xA, 2 * 448 * 2);
    const rsrc_t rWq = mkrs(isB ? wqB : wqA, 448 * 4096 * 2);
    const int row0 = 224 * (r8 >> 2) + 7 * (8 * w + (lane >> 3));
#pragma unroll
    for (int i = 0; i < 7; ++i) wqv[i] = ld16<AUX_NT>(rWq, row0 * 8192 + (lane & 7) * 16, i * 8192 + (h * 256 + (r8 & 3) * 64) * 2);
#pragma unroll
    for (int i = 0; i < 7; ++i) {
      xv[0][i] = __builtin_amdgcn_raw_buffer_load_b16(rX, row0 * 2 + i * 2, 0, 0);
      xv[1][i] = __builtin_amdgcn_raw_buffer_load_b16(rX, row0 * 2 + i * 2, 896, 0);
    }
    SBAR();
  }
  const u32 ovo = (w == 3 && l15 >= 8) ? 0x7fffff00u : (u32)(8 * g4 * 896 + w * 256 + l15 * 16);
  const int cr = r;
  const rsrc_t rUK = mkrs(wuk, 1024 * 4096 * 2), rUV = mkrs(wuv, 1024 * 4096 * 2);
  const u32 uvo = (16 * w + 8 * hf) * 8192 + rl * 16, us0 = 64 * cr * 8192 + h * 512;
  v4u wt[8], ta[16], tb[16];
#pragma unroll
  for (int i = 0; i < 8; ++i) wt[i] = ld16<AUX_NT>(rUK, uvo, us0 + i * 8192);
  SBAR();
  WGBAR();
  tag = __builtin_amdgcn_readfirstlane(ctl[2]);
  QSLICE()
  STAMP(1);
  slot(t0, T_Q);
  if (w == 3) QGATHER(WS_QA, vb, 0, 3)
  if (w == 1 || w == 2) QGATHER(WS_QB, w - 1, 128 * w, 5)
#define EXP_STOP 99
  lds_wait(ctl + 5, 2);
  STAMP(2);
  {
    const v4u q0 = *(LAS v4u*)(lds + L_Q + 128 + 4 * rl), q1 = *(LAS v4u*)(lds + L_Q + 256 + 4 * rl);
    float xs[16];
#pragma unroll
    for (int t = 0; t < 8; ++t) {
      float d = dot2(wt[t].x, q0.x, 0.f); d = dot2(wt[t].y, q0.y, d); d = dot2(wt[t].z, q0.z, d); xs[t] = dot2(wt[t].w, q0.w, d);
      d = dot2(wt[t].x, q1.x, 0.f); d = dot2(wt[t].y, q1.y, d); d = dot2(wt[t].z, q1.z, d); xs[8 + t] = dot2(wt[t].w, q1.w, d);
    }
    const float rr = red16x32(xs, lane);
    const float rn = dpp<XOR2>(rr);
    const int idx = (lane >> 1) & 15;
    if ((lane & 3) == 0) ((LAS u32*)(lds + L_S3S))[(idx >> 3) * 32 + 8 * w + 4 * hf + ((idx & 7) >> 1)] = cvtpk(rr, rn);
  }
  gbar(ctl, phase, lane);
  if (w == 0) st8(rW, WS_QABS + ((hf * 16 + h) * 512 + 32 * cr + rl) * 8, ((LAS u32*)(lds + L_S3S))[lane], tag);
  SBAR();
  STAMP(3);
  const rsrc_t rOA = mkrs(woA, 4096 * 448 * 2), rOB = mkrs(woB, 4096 * 448 * 2);
  v4u bfr[16], wob[8];
#define WO(i) { vmw<WO_WIN - 1>(); bfr[i] = ld16<0>(rOA, ovo, (h * 256 + 64 * dq + 32 * ((i) >> 3) + ((i) & 7)) * 896); SBAR(); }
  if (EXP_STOP > 1) {
    v4u g[16];
    const u32 qo = lane * 16, qs = WS_QABS + ab * 65536 + w * 1024;
    lds_wait(ctl + 8 + w, 1);
    slot(t0, T_A);
    auto ld = [&](int i) { return ld16<AUX_SC1>(rW, qo, qs + i * 4096); };
    gissue<16>(g, ld);
    SBAR();
#pragma unroll
    for (int i = 0; i < 8; ++i) wt[i] = ld16<AUX_NT>(rUV, uvo, us0 + i * 8192);
    SBAR();
#pragma unroll
    for (int i = 0; i < 16; ++i) ta[i] = *(LAS v4u*)(lds + L_T0 + ((w * 16 + i) * 64 + lane) * 4);
    gwait<16>(g, tag, ld);
    STAMP(4);
    LAS u32* qt = (LAS u32*)(lds + L_T0 + w * 4096);
    LGKM0();
#pragma unroll
    for (int i = 0; i < 16; ++i) *(LAS v2u*)(qt + i * 132 + 2 * lane) = v2u{g[i].x, g[i].z};
    LGKM0();
    v4u af[8];
#pragma unroll
    for (int ks = 0; ks < 8; ++ks) af[ks] = *(LAS v4u*)(qt + l15 * 132 + 16 * ks + 4 * g4);
    f32x4 s1[4] = {Z4, Z4, Z4, Z4}, s2[4] = {Z4, Z4, Z4, Z4};
#pragma unroll
    for (int ln = 0; ln < 4; ++ln) {
      const v4u a1 = af[2 * ln], a2 = af[2 * ln + 1];
#pragma unroll
      for (int rb = 0; rb < 4; ++rb) { s1[rb] = MFMA(a1, ta[4 * rb + ln], s1[rb]); s2[rb] = MFMA(a2, ta[4 * rb + ln], s2[rb]); }
    }
#pragma unroll
    for (int rb = 0; rb < 4; ++rb)
#pragma unroll
      for (int q = 0; q < 4; ++q)
        lds[L_S3A + ((2 * w + (l15 >> 3)) * 16 + 4 * g4 + q) * 36 + 8 * rb + (l15 & 7)] = (l15 & 8) ? s2[rb][q] : s1[rb][q];
    gbar(ctl, phase, lane);
    const int hh = 4 * w + g4;
    f32x2 sv = {0.f, 0.f};
#pragma unroll
    for (int ww = 0; ww < 8; ++ww) sv += *(LAS f32x2*)(lds + L_S3A + (ww * 16 + hh) * 36 + 2 * l15);
    sv *= 0.0625f;
    const float m = rowmax16(fmaxf(sv[0], sv[1]));
    const u32 pk = cvtpk(ex2((sv[0] - m) * LOG2E), ex2((sv[1] - m) * LOG2E));
    const float l = rowsum16(bflo(pk) + bfhi(pk));
    st8(rW, WS_PB + (((ab * 128 + kb) * 16 + hh) * 16 + l15) * 8, pk, tag);
    if (l15 == 0) st16(rW, WS_PBS + ((ab * 128 + kb) * 4 + w) * 128 + g4 * 16, asu(m), asu(l), tag);
    SBAR();
  }
  STAMP(5);
  gbar(ctl, phase, lane);
#pragma unroll
  for (int i = 0; i < WOA; ++i) WO(i)
  if (EXP_STOP > 2) {
    v4u g[6];
    const u32 so = l15 * 128 + g4 * 32, ss = WS_PB + (ab * 128 + 8 * kg + 2 * w) * 2048;
    const u32 to = g4 * 512 + (l15 >> 2) * 128 + (l15 & 3) * 16, ts = WS_PBS + (ab * 128 + 8 * kg) * 512;
    lds_wait(ctl + 8 + w, 2);
#pragma unroll
    for (int i = 0; i < 16; ++i) tb[i] = *(LAS v4u*)(lds + L_T1 + ((w * 16 + i) * 64 + lane) * 4);
    v4u bf[16];
#pragma unroll
    for (int i = 0; i < 16; ++i) { bf[i] = BFRAG(tb, 8 * (i >> 3), i & 7); asm volatile("" : "+v"(bf[i])); }
    slot(t0, T_P);
    auto ld = [&](int i) { return i < 4 ? ld16<AUX_SC1>(rW, so, ss + (i & 1) * 2048 + (i >> 1) * 16) : ld16<AUX_SC1>(rW, to, ts + (i & 1) * 2048); };
    gissue<6>(g, ld);
    gwait<6>(g, tag, ld);
    STAMP(6);
    const float M = mx32(mx16(fmaxf(asf(g[4].x), asf(g[5].x))));
    const float e0 = ex2((asf(g[4].x) - M) * LOG2E), e1 = ex2((asf(g[5].x) - M) * LOG2E);
    f32x4 ca[8];
#pragma unroll
    for (int pm = 0; pm < 2; ++pm) {
      const float sc = __shfl(w < 2 ? e0 : e1, ((2 * w + pm) & 3) * 16 + l15);
      const v4u pf = {cvtpk(sc * bflo(g[pm].x), sc * bfhi(g[pm].x)), cvtpk(sc * bflo(g[pm].z), sc * bfhi(g[pm].z)),
                      cvtpk(sc * bflo(g[2 + pm].x), sc * bfhi(g[2 + pm].x)), cvtpk(sc * bflo(g[2 + pm].z), sc * bfhi(g[2 + pm].z))};
#pragma unroll
      for (int i = 0; i < 8; ++i) ca[i] = MFMA(pf, bf[8 * pm + i], pm ? ca[i] : Z4);
    }
#pragma unroll
    for (int q = 0; q < 4; ++q) {
      LAS float* d = lds + L_S3B + (w * 16 + 4 * g4 + q) * 132 + 8 * l15;
      *(LAS f32x4*)d = f32x4{ca[0][q], ca[1][q], ca[2][q], ca[3][q]};
      *(LAS f32x4*)(d + 4) = f32x4{ca[4][q], ca[5][q], ca[6][q], ca[7][q]};
    }
    if (mm == 0 && w == 3) {
      float L = fmaf(e0, asf(g[4].z), e1 * asf(g[5].z)); L = tr16(L, L); L = tr32(L, L);
      if (lane < 16) st16(rW, WS_CPS + ((ab * 16 + kg) * 16 + lane) * 16, asu(M), asu(L), tag);
    }
    gbar(ctl, phase, lane);
#pragma unroll
    for (int i = 0; i < 4; ++i) {
      const int hh = w + 4 * i;
      f32x2 vv = {0.f, 0.f};
#pragma unroll
      for (int ww = 0; ww < 4; ++ww) vv += *(LAS f32x2*)(lds + L_S3B + (ww * 16 + hh) * 132 + 2 * lane);
      st8(rW, WS_CP + ((((ab * 16 + kg) * 8 + mm) * 16 + hh) * 64 + lane) * 8, cvtpk(vv[0], vv[1]), tag);
    }
    SBAR();
  }
  STAMP(7);
  gbar(ctl, phase, lane);
#pragma unroll
  for (int i = WOA; i < WOB; ++i) WO(i)
  if (EXP_STOP > 4) {
    v4u g0[3];
    const u32 co = g4 * (8 * 16 * 512) + l15 * 16, cs = WS_CP + ((4 * w * 8 + (cr >> 1)) * 16 + h) * 512 + (cr & 1) * 256;
    const u32 to = rl * 256, ts = WS_CPS + h * 16;
    slot(t0, T_C);
    auto ld = [&](int i) { return i < 2 ? ld16<AUX_SC1>(rW, co, cs + i * 1048576) : ld16<AUX_SC1>(rW, to, ts); };
    gissue<3>(g0, ld);
    SBAR();
    if (isB) {
#pragma unroll
      for (int i = 0; i < 8; ++i) wob[i] = ld16<AUX_NT>(rOB, ovo, (h * 256 + 32 * r8 + i) * 896);
    }
    SBAR();
    gwait<3>(g0, tag, ld);
    STAMP(8);
    const float M = rowmax16(asf(g0[2].x));
    float wg = ex2((asf(g0[2].x) - M) * LOG2E);
    wg /= rowsum16(wg * asf(g0[2].z));
    const float w0 = __shfl(wg, 4 * w + g4), w1 = __shfl(wg, 16 + 4 * w + g4);
    f32x4 cw = {tr32(w0 * bflo(g0[0].x), w1 * bflo(g0[1].x)), tr32(w0 * bfhi(g0[0].x), w1 * bfhi(g0[1].x)),
                tr32(w0 * bflo(g0[0].z), w1 * bflo(g0[1].z)), tr32(w0 * bfhi(g0[0].z), w1 * bfhi(g0[1].z))};
    cw = f32x4{tr16(cw[0], cw[0]), tr16(cw[1], cw[1]), tr16(cw[2], cw[2]), tr16(cw[3], cw[3])};
    if (!(g4 & 1)) *(LAS f32x4*)(lds + L_CW + (hf * 4 + w) * 64 + 4 * l15) = cw;
    gbar(ctl, phase, lane);
    float a2[2][8];
#pragma unroll
    for (int e = 0; e < 8; ++e) { a2[0][e] = 0.f; a2[1][e] = 0.f; }
#pragma unroll
    for (int b2 = 0; b2 < 2; ++b2) {
      const LAS float* cl = lds + L_CW + b2 * 256 + 16 * w + 8 * hf;
      f32x4 c0 = Z4, c1 = Z4;
#pragma unroll
      for (int ww = 0; ww < 4; ++ww) { c0 += *(const LAS f32x4*)(cl + ww * 64); c1 += *(const LAS f32x4*)(cl + ww * 64 + 4); }
#pragma unroll
      for (int t = 0; t < 8; ++t) {
        const float cv = t < 4 ? c0[t & 3] : c1[t & 3];
#pragma unroll
        for (int e = 0; e < 4; ++e) {
          a2[b2][2 * e] = fmaf(cv, bflo(wt[t][e]), a2[b2][2 * e]);
          a2[b2][2 * e + 1] = fmaf(cv, bfhi(wt[t][e]), a2[b2][2 * e + 1]);
        }
      }
    }
    float rr[8];
#pragma unroll
    for (int e = 0; e < 8; ++e) rr[e] = tr32(a2[0][e], a2[1][e]);
    LAS float* d = lds + L_XR + (w * 2 + hf) * 256 + 8 * rl;
    *(LAS f32x4*)d = f32x4{rr[0], rr[1], rr[2], rr[3]};
    *(LAS f32x4*)(d + 4) = f32x4{rr[4], rr[5], rr[6], rr[7]};
    gbar(ctl, phase, lane);
    f32x2 vv = {0.f, 0.f};
#pragma unroll
    for (int ww = 0; ww < 4; ++ww) vv += *(LAS f32x2*)(lds + L_XR + ww * 512 + 2 * (tid - 256));
    st8(rW, WS_XP + ((h * 16 + cr) * 256 + (tid - 256)) * 8, cvtpk(vv[0], vv[1]), tag);
    SBAR();
  }
  STAMP(9);
  gbar(ctl, phase, lane);
#pragma unroll
  for (int i = WOB; i < WO1; ++i) WO(i)
  u32 yb[2] = {0u, 0u};
  if (isB && EXP_STOP > 5) {
    v4u g[1];
    const u32 xo = WS_XP + (((h * 16 + 4 * w + g4) * 2 + (l15 >> 3)) * 128 + 16 * r8 + 2 * (l15 & 7)) * 8;
    slot(t0, T_X);
    auto ld = [&](int) { return ld16<AUX_SC1>(rW, xo, 0); };
    gissue<1>(g, ld);
    gwait<1>(g, tag, ld);
    STAMP(10);
    f32x4 v4 = {bflo(g[0].x), bfhi(g[0].x), bflo(g[0].z), bfhi(g[0].z)};
#pragma unroll
    for (int e = 0; e < 4; ++e) { v4[e] = tr32(v4[e], v4[e]); v4[e] = tr16(v4[e], v4[e]); }
    if (lane < 16) *(LAS f32x4*)(lds + L_S5 + w * 64 + 4 * l15) = v4;
    gbar(ctl, phase, lane);
    v4u af = {0u, 0u, 0u, 0u};
    if ((l15 & 2) == 0) {
      f32x4 lo = Z4, hi = Z4;
#pragma unroll
      for (int ww = 0; ww < 4; ++ww) {
        const LAS float* s = lds + L_S5 + ww * 64 + (l15 & 1) * 32 + 8 * g4;
        lo += *(const LAS f32x4*)s; hi += *(const LAS f32x4*)(s + 4);
      }
      af = v4u{cvtpk(lo[0], lo[1]), cvtpk(lo[2], lo[3]), cvtpk(hi[0], hi[1]), cvtpk(hi[2], hi[3])};
    }
    f32x4 d[8];
#pragma unroll
    for (int i = 0; i < 8; ++i) d[i] = MFMA(af, BFRAG(wob, 0, i), Z4);
    yb[0] = cvtpk(SEL4(d[0][0], d[2][0], d[4][0], d[6][0]), SEL4(d[1][0], d[3][0], d[5][0], d[7][0]));
    yb[1] = cvtpk(SEL4(d[0][1], d[2][1], d[4][1], d[6][1]), SEL4(d[1][1], d[3][1], d[5][1], d[7][1]));
    STAMP(11);
  }
#pragma unroll
  for (int i = WO1; i < 16; ++i) WO(i)
#pragma unroll
  for (int ks = 0; ks < 2; ++ks) {
    v4u t[8];
#pragma unroll
    for (int i = 0; i < 8; ++i) t[i] = BFRAG(bfr, 8 * ks, i);
#pragma unroll
    for (int i = 0; i < 8; ++i) { bfr[8 * ks + i] = t[i]; asm volatile("" : "+v"(bfr[8 * ks + i])); }
  }
  STAMP(12);
  {
    v4u g[4];
    const u32 so = lane * 16, ss = WS_SA + (bh * 16 + 4 * w) * 1024;
    gather_issue<4, 1024>(g, rW, so, ss);
    gather_wait_slow<4, 1024>(g, rW, so, ss, tag);
    float sc[16];
#pragma unroll
    for (int i = 0; i < 4; ++i) { sc[4 * i] = bflo(g[i].x); sc[4 * i + 1] = bfhi(g[i].x); sc[4 * i + 2] = bflo(g[i].z); sc[4 * i + 3] = bfhi(g[i].z); }
    float m = sc[0];
#pragma unroll
    for (int i = 1; i < 16; ++i) m = fmaxf(m, sc[i]);
    m = mx32(mx16(rowmax16(m))) * 0.0625f;
    float l = 0.f;
#pragma unroll
    for (int i = 0; i < 16; ++i) l += ex2((sc[i] * 0.0625f - m) * LOG2E);
    l = rowsum16(l); l = tr16(l, l); l = tr32(l, l);
    if (lane == 0) { lds[L_YB + w] = m; lds[L_YB + 4 + w] = l; }
    LGKM0();
    if (lane == 0) __hip_atomic_fetch_add(ctl + 6, 1u, __ATOMIC_RELAXED, __HIP_MEMORY_SCOPE_WORKGROUP);
  }
  STAMP(13);
  const rsrc_t rOut = mkrs(out, 2 * 896 * 2);
  const int ub = id >= 112, uc = id - 112 * ub;
#define FINAL_SUM(YBASE, COL0) { v4u g[2]; const u32 yo = lane * 2048, ys = (YBASE) + (ub * 128 * 256 + 2 * uc) * 8; \
    auto ld = [&](int i) { return ld16<AUX_SC1>(rW, yo, ys + i * 64 * 2048); }; gissue<2>(g, ld); gwait<2, TPOLL>(g, tag, ld); \
    float v0 = tr32(bflo(g[0].x) + bflo(g[1].x), bflo(g[0].z) + bflo(g[1].z)), v1 = tr32(bfhi(g[0].x) + bfhi(g[1].x), bfhi(g[0].z) + bfhi(g[1].z)); \
    v0 = rowsum16(tr16(v0, v0)); v1 = rowsum16(tr16(v1, v1)); \
    if (rl == 0) __builtin_amdgcn_raw_buffer_store_b32(cvtpk(v0, v1), rOut, (ub * 896 + (COL0) + 4 * uc + 2 * hf) * 2, 0, 0); }
  lds_wait(ctl + 4, 1);
  f32x4 d[8];
#pragma unroll
  for (int i = 0; i < 8; ++i) d[i] = Z4;
#pragma unroll
  for (int ks = 0; ks < 2; ++ks) {
    v4u a = {0u, 0u, 0u, 0u};
    if ((l15 & 2) == 0) a = *(LAS v4u*)(lds + L_OB + (l15 & 1) * 32 + 16 * ks + 4 * g4);
#pragma unroll
    for (int i = 0; i < 8; ++i) d[i] = MFMA(a, bfr[8 * ks + i], d[i]);
  }
  const float y0 = SEL4(d[0][0] + d[0][1], d[2][0] + d[2][1], d[4][0] + d[4][1], d[6][0] + d[6][1]);
  const float y1 = SEL4(d[1][0] + d[1][1], d[3][0] + d[3][1], d[5][0] + d[5][1], d[7][0] + d[7][1]);
  if (w < 3 || l15 < 8) {
    st8(rW, WS_YA + ((vb * 128 + h * 8 + r8) * 256 + 64 * w + 4 * l15 + g4) * 8, cvtpk(y0, y1), tag);
    if (isB) {
      const u32 yo2 = WS_YB + ((h * 8 + r8) * 256 + 64 * w + 4 * l15 + g4) * 8;
      st8(rW, yo2, yb[0], tag);
      st8(rW, yo2 + 128 * 256 * 8, yb[1], tag);
    }
  }
  STAMP(14);
  if (w == 1 && id < 224) FINAL_SUM(WS_YB, 448)
  if (w == 0 && id < 224) {
    FINAL_SUM(WS_YA, 0)
    if (id == 0 && lane == 0) __builtin_amdgcn_raw_buffer_store_b32(tag, rW, WS_EPOCH, 0, AUX_SC1);
  }
  STAMP(15);
  STAMP_DUMP();
}

extern "C" void kernel_launch(void* const* d_in, const int* in_sizes, int n_in, void* d_out, int out_size, void* d_ws,
                              size_t ws_size, hipStream_t stream) {
  fused_decode<<<256, 512, 0, stream>>>((const u16*)d_in[0], (const u16*)d_in[1], (const u16*)d_in[2], (const u16*)d_in[3],
                                        (const u16*)d_in[4], (const u16*)d_in[5], (const u16*)d_in[6], (const u16*)d_in[7],
                                        (const u16*)d_in[8], (const u16*)d_in[9], (const u16*)d_in[10], (u16*)d_out,
                                        (unsigned char*)d_ws);
}
```

```cpp
#include <hip/hip_runtime.h>
#include <stdint.h>

typedef unsigned u32;
typedef unsigned short u16;
typedef u32 v4u __attribute__((ext_vector_type(4)));
typedef u32 v2u __attribute__((ext_vector_type(2)));
typedef float f32x4 __attribute__((ext_vector_type(4)));
typedef float f32x2 __attribute__((ext_vector_type(2)));
typedef __bf16 bf16x2 __attribute__((ext_vector_type(2)));
typedef __bf16 bf16x8 __attribute__((ext_vector_type(8)));
typedef __amdgpu_buffer_rsrc_t rsrc_t;
#define LAS __attribute__((address_space(3)))

constexpr int WS_EPOCH = 0;
constexpr int WS_QA = 4096;
constexpr int WS_QB = WS_QA + 131072;
constexpr int WS_SA = WS_QB + 131072;
constexpr int WS_YA = WS_SA + 524288;
constexpr int WS_YB = WS_YA + 524288;
constexpr int WS_QABS = WS_YB + 1048576;
constexpr int WS_PB = WS_QABS + 131072;
constexpr int WS_PBS = WS_PB + 524288;
constexpr int WS_CP = WS_PBS + 131072;
constexpr int WS_CPS = WS_CP + 2097152;
constexpr int WS_END = WS_CPS + 8192;

constexpr int L_CTL = 0;
constexpr int L_Q = 16;
constexpr int L_PBUF = L_Q + 384;
constexpr int L_ORED = L_PBUF;
constexpr int L_OB = L_PBUF + 1024;
constexpr int L_S3S = L_OB + 128;
constexpr int L_YB = L_S3S + 256;
constexpr int L_STAMP = L_YB + 16;
constexpr int L_S3A = L_STAMP + 256;
constexpr int L_QRED = L_S3A;
constexpr int L_XR = L_S3A;
constexpr int L_CW = L_S3A + 512;
constexpr int L_T0 = L_S3A + 8 * 16 * 36;
constexpr int L_S3B = L_T0;
constexpr int L_T1 = L_T0 + 16384;
constexpr int L_TOTAL = L_T1 + 16384;
static_assert(L_TOTAL * 4 > 81920 && L_TOTAL * 4 <= 160 * 1024, "lds");

#define TPOLL 4
#define POLLS 10
constexpr int AUX_NT = 2, AUX_SC1 = 16;
constexpr float LOG2E = 1.4426950408889634f;
constexpr u32 SPIN_CAP = 1u << 17;

__device__ __forceinline__ rsrc_t mkrs(const void* p, u32 bytes) { return __builtin_amdgcn_make_buffer_rsrc((void*)p, 0, bytes, 0x00020000); }
template <int AUX> __device__ __forceinline__ v4u ld16(rsrc_t r, u32 voff, u32 soff) { return __builtin_amdgcn_raw_buffer_load_b128(r, voff, soff, AUX); }
__device__ __forceinline__ void st8(rsrc_t r, u32 off, u32 data, u32 tag) { __builtin_amdgcn_raw_buffer_store_b64(v2u{data, tag}, r, off, 0, AUX_SC1); }
__device__ __forceinline__ void st16(rsrc_t r, u32 off, u32 d0, u32 d1, u32 tag) { __builtin_amdgcn_raw_buffer_store_b128(v4u{d0, tag, d1, tag}, r, off, 0, AUX_SC1); }
__device__ __forceinline__ float asf(u32 x) { return __builtin_bit_cast(float, x); }
__device__ __forceinline__ u32 asu(float x) { return __builtin_bit_cast(u32, x); }
__device__ __forceinline__ float bflo(u32 x) { return asf(x << 16); }
__device__ __forceinline__ float bfhi(u32 x) { return asf(x & 0xffff0000u); }
__device__ __forceinline__ float dot2(u32 a, u32 b, float acc) {
  return __builtin_amdgcn_fdot2_f32_bf16(__builtin_bit_cast(bf16x2, a), __builtin_bit_cast(bf16x2, b), acc, false);
}
__device__ __forceinline__ u32 cvtpk(float lo, float hi) {
  bf16x2 r; r[0] = (__bf16)lo; r[1] = (__bf16)hi; return __builtin_bit_cast(u32, r);
}
__device__ __forceinline__ float ex2(float x) { return __builtin_amdgcn_exp2f(x); }
template <int CTRL> __device__ __forceinline__ float dpp(float x) {
  return asf((u32)__builtin_amdgcn_mov_dpp((int)asu(x), CTRL, 0xf, 0xf, true));
}
constexpr int XOR1 = 0xB1, XOR2 = 0x4E, XOR7 = 0x141, XOR8 = 0x128;
__device__ __forceinline__ float tr16(float a, float b) {
  auto s = __builtin_amdgcn_permlane16_swap(asu(a), asu(b), false, false); return asf(s[0]) + asf(s[1]);
}
__device__ __forceinline__ float tr32(float a, float b) {
  auto s = __builtin_amdgcn_permlane32_swap(asu(a), asu(b), false, false); return asf(s[0]) + asf(s[1]);
}
__device__ __forceinline__ float mx16(float a) { auto s = __builtin_amdgcn_permlane16_swap(asu(a), asu(a), false, false); return fmaxf(asf(s[0]), asf(s[1])); }
__device__ __forceinline__ float mx32(float a) { auto s = __builtin_amdgcn_permlane32_swap(asu(a), asu(a), false, false); return fmaxf(asf(s[0]), asf(s[1])); }
template <int CTRL> __device__ __forceinline__ float trd(float lo, float hi, bool bit) {
  const float keep = bit ? hi : lo, send = bit ? lo : hi; return keep + dpp<CTRL>(send);
}
__device__ __forceinline__ float rowsum16(float x) { x += dpp<XOR1>(x); x += dpp<XOR2>(x); x += dpp<XOR7>(x); x += dpp<XOR8>(x); return x; }
__device__ __forceinline__ float rowmax16(float x) { x = fmaxf(x, dpp<XOR1>(x)); x = fmaxf(x, dpp<XOR2>(x)); x = fmaxf(x, dpp<XOR7>(x)); x = fmaxf(x, dpp<XOR8>(x)); return x; }
__device__ __forceinline__ float red16x32(const float (&x)[16], int lane) {
  float y[8], z[4], u[2];
#pragma unroll
  for (int i = 0; i < 8; ++i) y[i] = tr16(x[i], x[i + 8]);
#pragma unroll
  for (int i = 0; i < 4; ++i) z[i] = trd<XOR8>(y[i], y[i + 4], lane & 8);
#pragma unroll
  for (int i = 0; i < 2; ++i) u[i] = trd<XOR7>(z[i], z[i + 2], lane & 4);
  float v = trd<XOR2>(u[0], u[1], lane & 2);
  return v + dpp<XOR1>(v);
}
__device__ __forceinline__ bool tags_ok(v4u g, u32 tag) { return g.y == tag && g.w == tag; }
__device__ __forceinline__ bool all_ok(bool ok) { return __builtin_amdgcn_ballot_w64(!ok) == 0ull; }
#define CBAR() asm volatile("" ::: "memory")
#define SBAR() __builtin_amdgcn_sched_barrier(0)
#define LGKM0() asm volatile("s_waitcnt lgkmcnt(0)" ::: "memory")

__device__ __forceinline__ void gbar(LAS u32* ctr, u32& phase, int lane) {
  phase += 4;
  LGKM0();
  if (lane == 0) __hip_atomic_fetch_add(ctr, 1u, __ATOMIC_RELAXED, __HIP_MEMORY_SCOPE_WORKGROUP);
#pragma unroll 1
  for (u32 s = 0; s < (1u << 24); ++s) {
    const u32 v = __builtin_amdgcn_readfirstlane(__hip_atomic_load(ctr, __ATOMIC_RELAXED, __HIP_MEMORY_SCOPE_WORKGROUP));
    if (v >= phase) break;
  }
  CBAR();
}
template <int N, int STRIDE> __device__ __forceinline__ void gather_wait(v4u (&g)[N], rsrc_t r, u32 voff, u32 soff, u32 tag) {
  u32 pend = 0;
#pragma unroll
  for (int i = 0; i < N; ++i) if (!all_ok(tags_ok(g[i], tag))) pend |= 1u << i;
  if (pend) {
    u32 s = 0;
    if (N > 4) {
      const u32 so = soff + (u32)__builtin_ctz(pend) * STRIDE;
#pragma unroll 1
      for (; s < SPIN_CAP; ++s) {
        __builtin_amdgcn_s_sleep(POLLS);
        asm volatile("" : "+v"(voff) :: "memory");
        const v4u c = ld16<AUX_SC1>(r, voff, so);
        if (all_ok(tags_ok(c, tag))) break;
      }
    }
    bool ok = false;
    if (N <= 4) __builtin_amdgcn_s_sleep(POLLS);
#pragma unroll 1
    for (; s < SPIN_CAP; ++s) {
      asm volatile("" : "+v"(voff) :: "memory");
      if (!ok) {
#pragma unroll
        for (int i = 0; i < N; ++i) g[i] = ld16<AUX_SC1>(r, voff, soff + i * STRIDE);
      }
      ok = true;
#pragma unroll
      for (int i = 0; i < N; ++i) ok = ok && tags_ok(g[i], tag);
      if (all_ok(ok)) break;
      __builtin_amdgcn_s_sleep(POLLS);
    }
  }
}
template <int N, int STRIDE> __device__ __forceinline__ void gather_wait_slow(v4u (&g)[N], rsrc_t r, u32 voff, u32 soff, u32 tag) {
#pragma unroll 1
  for (u32 s = 0; s < SPIN_CAP; ++s) {
    bool ok = true;
#pragma unroll
    for (int i = 0; i < N; ++i) ok = ok && tags_ok(g[i], tag);
    if (all_ok(ok)) break;
    __builtin_amdgcn_s_sleep(32);
    asm volatile("" : "+v"(voff) :: "memory");
    if (!ok) {
#pragma unroll
      for (int i = 0; i < N; ++i) g[i] = ld16<AUX_SC1>(r, voff, soff + i * STRIDE);
    }
  }
}
template <int N, int SL = POLLS, class F> __device__ __forceinline__ void gwait(v4u (&g)[N], u32 tag, F&& ld) {
#pragma unroll 1
  for (u32 s = 0; s < SPIN_CAP; ++s) {
    bool ok = true;
#pragma unroll
    for (int i = 0; i < N; ++i) ok = ok && tags_ok(g[i], tag);
    if (all_ok(ok)) break;
    __builtin_amdgcn_s_sleep(SL);
    CBAR();
    if (!ok) {
#pragma unroll
      for (int i = 0; i < N; ++i) g[i] = ld(i);
    }
  }
}
template <int N, class F> __device__ __forceinline__ void gissue(v4u (&g)[N], F&& ld) {
#pragma unroll
  for (int i = 0; i < N; ++i) g[i] = ld(i);
}
__device__ __forceinline__ void slot(unsigned long long t0, u32 ticks) {
#pragma unroll 1
  while ((u32)(__builtin_amdgcn_s_memrealtime() - t0) < ticks) __builtin_amdgcn_s_sleep(2);
}
template <int N, int STRIDE> __device__ __forceinline__ void gather_issue(v4u (&g)[N], rsrc_t r, u32 voff, u32 soff) {
#pragma unroll
  for (int i = 0; i < N; ++i) g[i] = ld16<AUX_SC1>(r, voff, soff + i * STRIDE);
}
__device__ __forceinline__ void lds_wait(LAS u32* flag, u32 want) {
#pragma unroll 1
  for (u32 s = 0; s < (1u << 24); ++s) {
    if ((u32)__builtin_amdgcn_readfirstlane(__hip_atomic_load(flag, __ATOMIC_RELAXED, __HIP_MEMORY_SCOPE_WORKGROUP)) >= want) break;
    __builtin_amdgcn_s_sleep(1);
  }
  CBAR();
}
#define STAMP(k) do { } while (0)
#define STAMP_INIT() do { } while (0)
#define STAMP_DUMP() do { } while (0)

#define DA 7
#define DPRE 3
#define WB 4
#define TB1 4
#define TB3 8
#define QSLEEP 16
#define TB2 8
#define WO_WIN 4
#define TA_AUX 0
#define TB_AUX 0
#define GPOS 8
#define WT_WIN 6
#define DS 5
#define NPRE 6
#define ASLEEP 32
#define PSLEEP 8
#define CSLEEP 16
#define WO1 16
#define T_Q 460
#define T_A 860
#define T_P 1560
#define T_C 2060
#define T_X 2160
#define WOA 6
#define WOB 12
#define WOS 72
#define WS1 40
#define WS2 68
#define WS3 84
#define G_T0 640
#define G_TAU 295
#define G_STEP 50
#define T_Y 2750
#define DA1 4
#define NSW 48
#define TB1U 4
#define WV_WIN 3
__host__ __device__ constexpr bool sideon(int n) { return (n > WS1 + 1 && n < WS1 + 18) || (n > WS2 + 1 && n < WS2 + 18) || (n > WS3 + 1 && n < WS3 + 18); }
__host__ __device__ constexpr int adist(int n) { return n < NSW ? DA1 : DA; }
template <int N> __device__ __forceinline__ void vmw() { asm volatile("s_waitcnt vmcnt(%0)" :: "i"(N) : "memory"); }
#define WGBAR() { LGKM0(); __builtin_amdgcn_s_barrier(); CBAR(); }
#define MFMA(A, B, C) __builtin_amdgcn_mfma_f32_16x16x32_bf16(__builtin_bit_cast(bf16x8, A), __builtin_bit_cast(bf16x8, B), C, 0, 0, 0)
#define PSEL(i) (((i) & 1) ? 0x07060302u : 0x05040100u)
#define BFRAG(T, O, i) v4u{__builtin_amdgcn_perm(T[(O) + 1][(i) >> 1], T[(O)][(i) >> 1], PSEL(i)), __builtin_amdgcn_perm(T[(O) + 3][(i) >> 1], T[(O) + 2][(i) >> 1], PSEL(i)), \
                           __builtin_amdgcn_perm(T[(O) + 5][(i) >> 1], T[(O) + 4][(i) >> 1], PSEL(i)), __builtin_amdgcn_perm(T[(O) + 7][(i) >> 1], T[(O) + 6][(i) >> 1], PSEL(i))}
#define SEL4(A0, A1, A2, A3) (g4 == 0 ? (A0) : g4 == 1 ? (A1) : g4 == 2 ? (A2) : (A3))
constexpr f32x4 Z4 = {0.f, 0.f, 0.f, 0.f};

#define QSLICE() { float a[16]; \
    _Pragma("unroll") for (int e = 0; e < 16; ++e) a[e] = 0.f; \
    _Pragma("unroll") for (int i = 0; i < 7; ++i) { \
      asm volatile("" : "+v"(wqv[i])); \
      const float x0 = bflo(xv[0][i]), x1 = bflo(xv[1][i]); \
      _Pragma("unroll") for (int e = 0; e < 4; ++e) { const float lo = bflo(wqv[i][e]), hi = bfhi(wqv[i][e]); \
        a[2 * e] = fmaf(x0, lo, a[2 * e]); a[2 * e + 1] = fmaf(x0, hi, a[2 * e + 1]); \
        a[8 + 2 * e] = fmaf(x1, lo, a[8 + 2 * e]); a[9 + 2 * e] = fmaf(x1, hi, a[9 + 2 * e]); } } \
    float a8[8], a4[4]; \
    _Pragma("unroll") for (int e = 0; e < 8; ++e) a8[e] = tr32(a[e], a[e + 8]);              \
    _Pragma("unroll") for (int e = 0; e < 4; ++e) a4[e] = tr16(a8[e], a8[e + 4]);            \
    const float c0 = trd<XOR8>(a4[0], a4[2], lane & 8), c1 = trd<XOR8>(a4[1], a4[3], lane & 8);     \
    *(LAS f32x2*)(lds + L_QRED + w * 128 + hf * 64 + (lane & 7) * 8 + (g4 & 1) * 4 + ((lane >> 3) & 1) * 2) = f32x2{c0, c1}; \
    gbar(ctl, phase, lane); \
    if (w == 0) { f32x2 s = {0.f, 0.f}; \
      _Pragma("unroll") for (int ww = 0; ww < 4; ++ww) s += *(LAS f32x2*)(lds + L_QRED + ww * 128 + 2 * lane); \
      st16(rW, (isB ? WS_QB : WS_QA) + ((((h * 2 + hf) * 2 + qk) * 256) + qb * 64 + 2 * rl) * 8, asu(s[0]), asu(s[1]), tag); } }
#define QGATHER(BASE, BB, LQ, FLAG) { v4u g[4]; const u32 qo = lane * 16, qs = (BASE) + (h * 2 + (BB)) * 4096; \
    auto ld = [&](int i) { return ld16<AUX_SC1>(rW, qo, qs + i * 1024); }; gissue<4>(g, ld); gwait<4>(g, tag, ld); \
    ((LAS u32*)(lds + L_Q))[(LQ) + lane] = cvtpk(asf(g[0].x) + asf(g[2].x), asf(g[0].z) + asf(g[2].z)); \
    ((LAS u32*)(lds + L_Q))[(LQ) + 64 + lane] = cvtpk(asf(g[1].x) + asf(g[3].x), asf(g[1].z) + asf(g[3].z)); \
    LGKM0(); \
    if (lane == 0) __hip_atomic_fetch_add(ctl + (FLAG), 1u, __ATOMIC_RELAXED, __HIP_MEMORY_SCOPE_WORKGROUP); }
#define KDOT(s) { float d = dot2(c.x, qp[0], 0.f); d = dot2(c.y, qp[1], d); d = dot2(c.z, qp[2], d); xs[s] = dot2(c.w, qp[3], d); \
    asm volatile("" : "+v"(xs[s])); SBAR(); }
#define PUBLISH_SCORES(SG, SA_, SB_) { const int idx = (lane >> 1) & 15; \
    if (!(lane & 1)) st8(rW, WS_SA + (((bh * 16 + (SG)) * 4 + w) * 32 + (idx & 1) * 16 + hf * 8 + (idx >> 1)) * 8, cvtpk(SA_, SB_), tag); }
__global__ void __launch_bounds__(512, 2)
fused_decode(const u16* __restrict__ xA, const u16* __restrict__ Kc, const u16* __restrict__ Vc, const u16* __restrict__ wqA,
             const u16* __restrict__ woA, const u16* __restrict__ xB, const u16* __restrict__ ckv, const u16* __restrict__ wqB,
             const u16* __restrict__ wuk, const u16* __restrict__ wuv, const u16* __restrict__ woB, u16* out,
             unsigned char* ws) {
  __shared__ __attribute__((aligned(16))) float lds_[L_TOTAL];
  LAS float* lds = (LAS float*)lds_;
  LAS u32* ctl = (LAS u32*)lds_;
  const int tid = threadIdx.x, lane = tid & 63;
  const int wave = __builtin_amdgcn_readfirstlane(tid >> 6), hw = wave >> 2, w = wave & 3;
  const int id = blockIdx.x, x = id & 7, y = id >> 3;
  const int h = x + 8 * (y >> 4), r = y & 15, r8 = r & 7;
  const bool isB = r < 8;
  const int hf = lane >> 5, rl = lane & 31, l15 = lane & 15, g4 = lane >> 4;
  const int vb = r >> 3, dh = r8 >> 2, vu = r8 & 3, bh = vb * 16 + h;
  const int dq = (r8 >> 1) ^ 2, cq = (r & 1) + 2 * (r >> 3);
  const int qb = 3 - (r8 >> 1), qk = r8 & 1;
  const int grp = x * 4 + (y >> 3), ab = grp >> 4, kg = grp & 15, mm = r8, kb = 8 * kg + mm;
  const rsrc_t rW = mkrs(ws, WS_END);
  const unsigned long long t0 = __builtin_amdgcn_s_memrealtime();
  STAMP_INIT();
  STAMP(0);
  u32 tag, phase = 0;

  if (hw == 0) {
    const rsrc_t rK = mkrs(Kc, 64u << 20), rV = mkrs(Vc, 64u << 20);
    const u32 kvo = w * 1024 + lane * 16, vvo = (4 * w + g4) * 512 + l15 * 16;
    const u32 ks0 = (u32)(bh * 4096 + 256 * r8) * 512;
    const u32 vs0 = (u32)(bh * 4096 + 512 * vu) * 512 + dh * 256;
    v4u buf[16];
    u32 ep = __builtin_amdgcn_raw_buffer_load_b32(rW, WS_EPOCH, 0, AUX_SC1);
    SBAR();
#define ISSUE(m) { if ((m) < 64) buf[(m) & 15] = ld16<AUX_NT>(rK, kvo, ks0 + ((m) >> 5) * (2048 * 512) + ((m) & 31) * 4096); \
      else if ((m) < 128) buf[(m) & 15] = ld16<AUX_NT>(rV, vvo, vs0 + (((m) >> 5) & 1) * (2048 * 512) + ((m) & 31) * 8192); }
    {
      const rsrc_t rC = mkrs(ckv, 2 * 4096 * 1024 * 2);
      const u32 avo = (l15 & 7) * 2048 + w * 512 + (l15 >> 3) * 64 + g4 * 16, as0 = (u32)(ab * 4096 + 32 * kb) * 2048;
      const u32 bvo = (64 * w + 8 * g4) * 2048 + l15 * 16, bs0 = (u32)(ab * 4096 + 256 * kg) * 2048 + r8 * 256;
#define TISSUE(m) { if ((m) < 16) sd[(m) % DS] = ld16<0>(rC, avo + ((m) & 3) * 128, as0 + ((m) >> 2) * 8 * 2048); \
        else if ((m) < 32) sd[(m) % DS] = ld16<0>(rC, bvo, bs0 + ((((m) >> 3) & 1) * 32 + ((m) & 7)) * 2048); }
      v4u sd[DS];
#pragma unroll
      for (int m = 0; m < DS; ++m) TISSUE(m)
      SBAR();
      asm volatile("" : "+v"(ep));
      if (tid == 0) { ctl[0] = 0; ctl[1] = 0; ctl[2] = ep + 1u; ctl[3] = 0; ctl[4] = 0; ctl[5] = 0; ctl[6] = 0; ctl[7] = 0; ctl[8] = 0; ctl[9] = 0; ctl[10] = 0; ctl[11] = 0; }
      WGBAR();
      tag = __builtin_amdgcn_readfirstlane(ctl[2]);
#pragma unroll
      for (int m = 0; m < 32; ++m) {
        const v4u c = sd[m % DS];
        TISSUE(m + DS)
        if (m >= 32 - NPRE) ISSUE(m - (32 - NPRE))
        *(LAS v4u*)(lds + (m < 16 ? L_T0 : L_T1) + ((w * 16 + (m & 15)) * 64 + lane) * 4) = c;
        if (m == 15 || m == 31) { LGKM0(); if (lane == 0) __hip_atomic_store(ctl + 8 + w, (u32)(m >> 4) + 1u, __ATOMIC_RELAXED, __HIP_MEMORY_SCOPE_WORKGROUP); }
        SBAR();
      }
    }
    STAMP(6);
    lds_wait(ctl + 3, 1);
    const v4u qp = *(LAS v4u*)(lds + L_Q + 4 * rl);
    STAMP(1);
    int gmode = 1;
#define GOVERN(n) { if ((n) >= NSW) { \
        if (((n) & 7) == 0) { const int lag = (int)(u32)(__builtin_amdgcn_s_memrealtime() - t0) - (G_T0 + (((n) * G_TAU) >> 4)); \
          gmode = lag < -G_STEP ? 0 : lag < 0 ? 1 : lag < G_STEP ? 2 : 3; } \
        if (sideon(n)) { if (gmode == 0) vmw<2 * (DA - 4) - 1>(); else if (gmode == 1) vmw<2 * (DA - 3) - 1>(); else if (gmode == 2) vmw<2 * (DA - 2) - 1>(); } \
        else { if (gmode == 0) vmw<DA - 4>(); else if (gmode == 1) vmw<DA - 3>(); else if (gmode == 2) vmw<DA - 2>(); } } }
#define REFILL(n) { GOVERN(n) if ((n) < DA1) { ISSUE((n) + NPRE) } else if ((n) >= NPRE) { \
      _Pragma("unroll") for (int m_ = (n) + adist((n) - 1); m_ <= (n) + adist(n); ++m_) { ISSUE(m_) } } }
#define PPOS(i) (16 * ((i) >> 4) + 2 * ((i) & 7) + (((i) >> 3) & 1))
    const u32 go_s = WS_SA + (((bh * 16 + 2 * vu + ((rl >> 2) & 1)) * 4 + 2 * (w & 1) + (rl >> 4)) * 32 + (w >> 1) * 16 + ((rl >> 3) & 1) * 8 + 2 * (rl & 3)) * 8;
    const rsrc_t rOA = mkrs(woA, 4096 * 448 * 2), rOB = mkrs(woB, 4096 * 448 * 2);
    const u32 ovo = (w == 3 && l15 >= 8) ? 0x7fffff00u : (u32)(8 * g4 * 896 + w * 256 + l15 * 16);
    v4u sw[8];
#define SIDE1(n, S, RS, ROW0, DST, NEED, DONE) { \
      if ((n) >= (S) && (n) < (S) + 16) { const int j_ = (n) - (S); sw[(n) & 7] = ld16<0>(RS, ovo, ((ROW0) + 32 * (j_ >> 3) + (j_ & 7)) * 896); } \
      if ((n) >= (S) + 7 && (n) < (S) + 23) { const int j_ = (n) - (S) - 7; \
        if (j_ == 0) lds_wait(ctl + 7, NEED); \
        *(LAS v4u*)(lds + (DST) + ((w * 16 + j_) * 64 + lane) * 4) = sw[((n) - 7) & 7]; \
        if (j_ == 15) { LGKM0(); if (lane == 0) __hip_atomic_store(ctl + 8 + w, DONE, __ATOMIC_RELAXED, __HIP_MEMORY_SCOPE_WORKGROUP); } } }
#define SIDE(n) { SIDE1(n, WS1, rOB, h * 256 + 64 * dq, L_T1, 4, 3u) SIDE1(n, WS2, rOA, h * 256 + 128 * dh, L_T0, 8, 4u) \
      SIDE1(n, WS3, rOA, h * 256 + 128 * dh + 64, L_T1, 12, 5u) }
    v4u gs[1];
    {
      float xs[16], sa, sb;
#pragma unroll
      for (int sl = 0; sl < 2; ++sl) {
#pragma unroll
        for (int s = 0; s < 16; ++s) { const v4u c = buf[s]; REFILL(32 * sl + s) SIDE(32 * sl + s) KDOT(s) }
        sa = red16x32(xs, lane);
#pragma unroll
        for (int s = 0; s < 16; ++s) {
          if (sl == 1 && s == 16 - GPOS) { gather_issue<1, 0>(gs, rW, go_s, 0); SBAR(); }
          const v4u c = buf[s]; REFILL(32 * sl + 16 + s) SIDE(32 * sl + 16 + s) KDOT(s)
        }
        sb = red16x32(xs, lane);
        PUBLISH_SCORES(8 * sl + r8, sa, sb)
        STAMP(2 + sl);
      }
    }
    LAS float* pb = lds + L_PBUF + w * 256;
    float acc[8], pv[32], mrun = -INFINITY;
#pragma unroll
    for (int e = 0; e < 8; ++e) acc[e] = 0.f;
#define MAKE_P() { \
    const float s0 = bflo(gs[0].x) * 0.0625f, s1 = bfhi(gs[0].x) * 0.0625f, s2 = bflo(gs[0].z) * 0.0625f, s3 = bfhi(gs[0].z) * 0.0625f; \
    const float mn = fmaxf(mrun, mx16(rowmax16(fmaxf(fmaxf(s0, s1), fmaxf(s2, s3))))); \
    const float sc = ex2((mrun - mn) * LOG2E); mrun = mn; \
    _Pragma("unroll") for (int e = 0; e < 8; ++e) acc[e] *= sc; \
    if (lane < 32) *(LAS f32x4*)(pb + lane * 4) = f32x4{ex2((s0 - mn) * LOG2E), ex2((s1 - mn) * LOG2E), ex2((s2 - mn) * LOG2E), ex2((s3 - mn) * LOG2E)}; \
    LGKM0(); \
    _Pragma("unroll") for (int u = 0; u < 8; ++u) { const f32x4 t = *(LAS f32x4*)(pb + g4 * 32 + u * 4); \
      pv[4 * u] = t[0]; pv[4 * u + 1] = t[1]; pv[4 * u + 2] = t[2]; pv[4 * u + 3] = t[3]; } \
    LGKM0(); }
#define VSTEPS(N0, S0, S1) _Pragma("unroll") for (int s = (S0); s < (S1); ++s) { const v4u c = buf[s & 15]; REFILL((N0) + s) SIDE((N0) + s) \
      const float p = pv[PPOS(s)]; \
      _Pragma("unroll") for (int e = 0; e < 4; ++e) { acc[2 * e] = fmaf(p, bflo(c[e]), acc[2 * e]); acc[2 * e + 1] = fmaf(p, bfhi(c[e]), acc[2 * e + 1]); } \
      asm volatile("" : "+v"(acc[0]), "+v"(acc[1]), "+v"(acc[2]), "+v"(acc[3]), "+v"(acc[4]), "+v"(acc[5]), "+v"(acc[6]), "+v"(acc[7])); SBAR(); }
    gather_wait<1, 0>(gs, rW, go_s, 0, tag);
    MAKE_P()
    VSTEPS(64, 0, 32 - GPOS)
    gather_issue<1, 0>(gs, rW, go_s, 8192);
    SBAR();
    VSTEPS(64, 32 - GPOS, 32)
    STAMP(4);
    gather_wait<1, 0>(gs, rW, go_s, 8192, tag);
    MAKE_P()
    VSTEPS(96, 0, 32)
    lds_wait(ctl + 6, 4);
    float fin;
    {
      const f32x4 m4 = *(LAS f32x4*)(lds + L_YB), l4 = *(LAS f32x4*)(lds + L_YB + 4);
      const float M = fmaxf(fmaxf(m4[0], m4[1]), fmaxf(m4[2], m4[3]));
      const float L = (l4[0] * ex2((m4[0] - M) * LOG2E) + l4[1] * ex2((m4[1] - M) * LOG2E)) + (l4[2] * ex2((m4[2] - M) * LOG2E) + l4[3] * ex2((m4[3] - M) * LOG2E));
      fin = ex2((mrun - M) * LOG2E) / L;
    }
    float r4[4];
#pragma unroll
    for (int e = 0; e < 4; ++e) r4[e] = tr32(acc[e], acc[e + 4]);
    const float r0 = tr16(r4[0], r4[2]) * fin, r1 = tr16(r4[1], r4[3]) * fin;
    *(LAS f32x2*)(lds + L_ORED + w * 256 + 8 * l15 + 4 * hf + 2 * (g4 & 1)) = f32x2{r0, r1};
    LGKM0();
    u32 old = 0;
    if (lane == 0) old = __hip_atomic_fetch_add(ctl + 1, 1u, __ATOMIC_RELAXED, __HIP_MEMORY_SCOPE_WORKGROUP);
    old = __builtin_amdgcn_readfirstlane(old);
    CBAR();
    if (old == 3) {
      f32x2 o = {0.f, 0.f};
#pragma unroll
      for (int ww = 0; ww < 4; ++ww) o += *(LAS f32x2*)(lds + L_ORED + ww * 256 + 2 * lane);
      const u32 hi = cvtpk(o[0], o[1]);
      ((LAS u32*)(lds + L_OB))[lane] = hi;
      ((LAS u32*)(lds + L_OB))[64 + lane] = cvtpk(o[0] - bflo(hi), o[1] - bfhi(hi));
      LGKM0();
      if (lane == 0) __hip_atomic_store(ctl + 4, 1u, __ATOMIC_RELAXED, __HIP_MEMORY_SCOPE_WORKGROUP);
    }
    STAMP(5);
    STAMP_DUMP();
    return;
  }

  v4u wqv[7]; u32 xv[2][7];
  {
    const rsrc_t rX = mkrs(isB ? xB : xA, 2 * 448 * 2);
    const rsrc_t rWq = mkrs(isB ? wqB : wqA, 448 * 4096 * 2);
    const int row0 = 224 * qk + 7 * (8 * w + (lane >> 3));
#pragma unroll
    for (int i = 0; i < 7; ++i) wqv[i] = ld16<AUX_NT>(rWq, row0 * 8192 + (lane & 7) * 16, i * 8192 + (h * 256 + qb * 64) * 2);
#pragma unroll
    for (int i = 0; i < 7; ++i) {
      xv[0][i] = __builtin_amdgcn_raw_buffer_load_b16(rX, row0 * 2 + i * 2, 0, 0);
      xv[1][i] = __builtin_amdgcn_raw_buffer_load_b16(rX, row0 * 2 + i * 2, 896, 0);
    }
    SBAR();
  }
  const u32 ovo = (w == 3 && l15 >= 8) ? 0x7fffff00u : (u32)(8 * g4 * 896 + w * 256 + l15 * 16);
  const int cr = r;
  const rsrc_t rUK = mkrs(wuk, 1024 * 4096 * 2), rUV = mkrs(wuv, 1024 * 4096 * 2);
  const u32 uvo = (16 * w + 8 * hf) * 8192 + rl * 16, us0 = 64 * cr * 8192 + h * 512;
  v4u wt[8], ta[16], tb[16];
#pragma unroll
  for (int i = 0; i < 8; ++i) wt[i] = ld16<AUX_NT>(rUK, uvo, us0 + i * 8192);
  SBAR();
  WGBAR();
  tag = __builtin_amdgcn_readfirstlane(ctl[2]);
  QSLICE()
  STAMP(1);
  slot(t0, T_Q);
  if (w == 3) QGATHER(WS_QA, vb, 0, 3)
  if (w == 1 || w == 2) QGATHER(WS_QB, w - 1, 128 * w, 5)
#define EXP_STOP 99
  lds_wait(ctl + 5, 2);
  STAMP(2);
  {
    const v4u q0 = *(LAS v4u*)(lds + L_Q + 128 + 4 * rl), q1 = *(LAS v4u*)(lds + L_Q + 256 + 4 * rl);
    float xs[16];
#pragma unroll
    for (int t = 0; t < 8; ++t) {
      float d = dot2(wt[t].x, q0.x, 0.f); d = dot2(wt[t].y, q0.y, d); d = dot2(wt[t].z, q0.z, d); xs[t] = dot2(wt[t].w, q0.w, d);
      d = dot2(wt[t].x, q1.x, 0.f); d = dot2(wt[t].y, q1.y, d); d = dot2(wt[t].z, q1.z, d); xs[8 + t] = dot2(wt[t].w, q1.w, d);
    }
    const float rr = red16x32(xs, lane);
    const float rn = dpp<XOR2>(rr);
    const int idx = (lane >> 1) & 15;
    if ((lane & 3) == 0) ((LAS u32*)(lds + L_S3S))[(idx >> 3) * 32 + 8 * w + 4 * hf + ((idx & 7) >> 1)] = cvtpk(rr, rn);
  }
  gbar(ctl, phase, lane);
  if (w == 0) st8(rW, WS_QABS + ((hf * 16 + h) * 512 + 32 * cr + rl) * 8, ((LAS u32*)(lds + L_S3S))[lane], tag);
  SBAR();
  STAMP(3);
  v4u bfr[32];
#define WO_UNUSED(i) { vmw<WO_WIN - 1>(); bfr[i] = ld16<0>(rOA, ovo, (h * 256 + 64 * dq + 32 * ((i) >> 3) + ((i) & 7)) * 896); SBAR(); }
  if (EXP_STOP > 1) {
    v4u g[16];
    const u32 qo = lane * 16, qs = WS_QABS + ab * 65536 + w * 1024;
    lds_wait(ctl + 8 + w, 1);
    slot(t0, T_A);
    auto ld = [&](int i) { return ld16<AUX_SC1>(rW, qo, qs + i * 4096); };
    gissue<16>(g, ld);
    SBAR();
#pragma unroll
    for (int i = 0; i < 8; ++i)
      wt[i] = ld16<AUX_NT>(rUV, (64 * w + (lane >> 3)) * 8192 + (lane & 7) * 16, (256 * cq + 8 * i) * 8192 + h * 512 + dq * 128);
    SBAR();
#pragma unroll
    for (int i = 0; i < 16; ++i) ta[i] = *(LAS v4u*)(lds + L_T0 + ((w * 16 + i) * 64 + lane) * 4);
    gwait<16>(g, tag, ld);
    STAMP(4);
    LAS u32* qt = (LAS u32*)(lds + L_T0 + w * 4096);
    LGKM0();
#pragma unroll
    for (int i = 0; i < 16; ++i) *(LAS v2u*)(qt + i * 132 + 2 * lane) = v2u{g[i].x, g[i].z};
    LGKM0();
    v4u af[8];
#pragma unroll
    for (int ks = 0; ks < 8; ++ks) af[ks] = *(LAS v4u*)(qt + l15 * 132 + 16 * ks + 4 * g4);
    f32x4 s1[4] = {Z4, Z4, Z4, Z4}, s2[4] = {Z4, Z4, Z4, Z4};
#pragma unroll
    for (int ln = 0; ln < 4; ++ln) {
      const v4u a1 = af[2 * ln], a2 = af[2 * ln + 1];
#pragma unroll
      for (int rb = 0; rb < 4; ++rb) { s1[rb] = MFMA(a1, ta[4 * rb + ln], s1[rb]); s2[rb] = MFMA(a2, ta[4 * rb + ln], s2[rb]); }
    }
#pragma unroll
    for (int rb = 0; rb < 4; ++rb)
#pragma unroll
      for (int q = 0; q < 4; ++q)
        lds[L_S3A + ((2 * w + (l15 >> 3)) * 16 + 4 * g4 + q) * 36 + 8 * rb + (l15 & 7)] = (l15 & 8) ? s2[rb][q] : s1[rb][q];
    gbar(ctl, phase, lane);
    const int hh = 4 * w + g4;
    f32x2 sv = {0.f, 0.f};
#pragma unroll
    for (int ww = 0; ww < 8; ++ww) sv += *(LAS f32x2*)(lds + L_S3A + (ww * 16 + hh) * 36 + 2 * l15);
    sv *= 0.0625f;
    const float m = rowmax16(fmaxf(sv[0], sv[1]));
    const u32 pk = cvtpk(ex2((sv[0] - m) * LOG2E), ex2((sv[1] - m) * LOG2E));
    const float l = rowsum16(bflo(pk) + bfhi(pk));
    st8(rW, WS_PB + (((ab * 128 + kb) * 16 + hh) * 16 + l15) * 8, pk, tag);
    if (l15 == 0) st16(rW, WS_PBS + ((ab * 128 + kb) * 4 + w) * 128 + g4 * 16, asu(m), asu(l), tag);
    SBAR();
  }
  STAMP(5);
  gbar(ctl, phase, lane);
  if (EXP_STOP > 2) {
    v4u g[6];
    const u32 so = l15 * 128 + g4 * 32, ss = WS_PB + (ab * 128 + 8 * kg + 2 * w) * 2048;
    const u32 to = g4 * 512 + (l15 >> 2) * 128 + (l15 & 3) * 16, ts = WS_PBS + (ab * 128 + 8 * kg) * 512;
    lds_wait(ctl + 8 + w, 2);
#pragma unroll
    for (int i = 0; i < 16; ++i) tb[i] = *(LAS v4u*)(lds + L_T1 + ((w * 16 + i) * 64 + lane) * 4);
    v4u bf[16];
#pragma unroll
    for (int i = 0; i < 16; ++i) { bf[i] = BFRAG(tb, 8 * (i >> 3), i & 7); asm volatile("" : "+v"(bf[i])); }
    if (lane == 0) __hip_atomic_fetch_add(ctl + 7, 1u, __ATOMIC_RELAXED, __HIP_MEMORY_SCOPE_WORKGROUP);
    slot(t0, T_P);
    auto ld = [&](int i) { return i < 4 ? ld16<AUX_SC1>(rW, so, ss + (i & 1) * 2048 + (i >> 1) * 16) : ld16<AUX_SC1>(rW, to, ts + (i & 1) * 2048); };
    gissue<6>(g, ld);
    gwait<6>(g, tag, ld);
    STAMP(6);
    const float M = mx32(mx16(fmaxf(asf(g[4].x), asf(g[5].x))));
    const float e0 = ex2((asf(g[4].x) - M) * LOG2E), e1 = ex2((asf(g[5].x) - M) * LOG2E);
    f32x4 ca[8];
#pragma unroll
    for (int pm = 0; pm < 2; ++pm) {
      const float sc = __shfl(w < 2 ? e0 : e1, ((2 * w + pm) & 3) * 16 + l15);
      const v4u pf = {cvtpk(sc * bflo(g[pm].x), sc * bfhi(g[pm].x)), cvtpk(sc * bflo(g[pm].z), sc * bfhi(g[pm].z)),
                      cvtpk(sc * bflo(g[2 + pm].x), sc * bfhi(g[2 + pm].x)), cvtpk(sc * bflo(g[2 + pm].z), sc * bfhi(g[2 + pm].z))};
#pragma unroll
      for (int i = 0; i < 8; ++i) ca[i] = MFMA(pf, bf[8 * pm + i], pm ? ca[i] : Z4);
    }
#pragma unroll
    for (int q = 0; q < 4; ++q) {
      LAS float* d = lds + L_S3B + (w * 16 + 4 * g4 + q) * 132 + 8 * l15;
      *(LAS f32x4*)d = f32x4{ca[0][q], ca[1][q], ca[2][q], ca[3][q]};
      *(LAS f32x4*)(d + 4) = f32x4{ca[4][q], ca[5][q], ca[6][q], ca[7][q]};
    }
    if (mm == 0 && w == 3) {
      float L = fmaf(e0, asf(g[4].z), e1 * asf(g[5].z)); L = tr16(L, L); L = tr32(L, L);
      if (lane < 16) st16(rW, WS_CPS + ((ab * 16 + kg) * 16 + lane) * 16, asu(M), asu(L), tag);
    }
    gbar(ctl, phase, lane);
#pragma unroll
    for (int i = 0; i < 4; ++i) {
      const int hh = w + 4 * i;
      f32x2 vv = {0.f, 0.f};
#pragma unroll
      for (int ww = 0; ww < 4; ++ww) vv += *(LAS f32x2*)(lds + L_S3B + (ww * 16 + hh) * 132 + 2 * lane);
      st8(rW, WS_CP + ((((ab * 16 + kg) * 8 + mm) * 16 + hh) * 64 + lane) * 8, cvtpk(vv[0], vv[1]), tag);
    }
    SBAR();
  }
  STAMP(7);
  gbar(ctl, phase, lane);
  if (lane == 0) __hip_atomic_fetch_add(ctl + 7, 1u, __ATOMIC_RELAXED, __HIP_MEMORY_SCOPE_WORKGROUP);
  if (EXP_STOP > 4) {
    v4u g0[9];
    const int c64 = 4 * cq + w;
    const u32 co = g4 * (8 * 16 * 512) + l15 * 16, cs = WS_CP + ((c64 >> 1) * 16 + h) * 512 + (c64 & 1) * 256;
    const u32 to = rl * 256, ts = WS_CPS + h * 16;
    lds_wait(ctl + 8 + w, 3);
#pragma unroll
    for (int ks = 0; ks < 2; ++ks) {
      v4u t[8];
#pragma unroll
      for (int i = 0; i < 8; ++i) t[i] = *(LAS v4u*)(lds + L_T1 + ((w * 16 + 8 * ks + i) * 64 + lane) * 4);
#pragma unroll
      for (int i = 0; i < 8; ++i) { bfr[8 * ks + i] = BFRAG(t, 0, i); asm volatile("" : "+v"(bfr[8 * ks + i])); }
    }
    if (lane == 0) __hip_atomic_fetch_add(ctl + 7, 1u, __ATOMIC_RELAXED, __HIP_MEMORY_SCOPE_WORKGROUP);
    slot(t0, T_C);
    auto ld = [&](int i) { return i < 8 ? ld16<AUX_SC1>(rW, co, cs + i * (4 * 8 * 16 * 512)) : ld16<AUX_SC1>(rW, to, ts); };
    gissue<9>(g0, ld);
    gwait<9>(g0, tag, ld);
    STAMP(8);
    const float M = rowmax16(asf(g0[8].x));
    float wg = ex2((asf(g0[8].x) - M) * LOG2E);
    wg /= rowsum16(wg * asf(g0[8].z));
    f32x4 cb[2] = {Z4, Z4};
#pragma unroll
    for (int i = 0; i < 8; ++i) {
      const float wi = __shfl(wg, 4 * i + g4);
      cb[i >> 2] += wi * f32x4{bflo(g0[i].x), bfhi(g0[i].x), bflo(g0[i].z), bfhi(g0[i].z)};
    }
    f32x4 cw;
#pragma unroll
    for (int e = 0; e < 4; ++e) { cw[e] = tr32(cb[0][e], cb[1][e]); cw[e] = tr16(cw[e], cw[e]); }
    LAS float* cl = lds + L_CW + w * 128;
    if (!(g4 & 1)) *(LAS f32x4*)(cl + hf * 64 + 4 * l15) = cw;
    LGKM0();
    float a2[16];
#pragma unroll
    for (int e = 0; e < 16; ++e) a2[e] = 0.f;
#pragma unroll
    for (int i = 0; i < 8; ++i) {
      const float c0 = cl[8 * i + (lane >> 3)], c1 = cl[64 + 8 * i + (lane >> 3)];
#pragma unroll
      for (int e = 0; e < 4; ++e) {
        const float lo = bflo(wt[i][e]), hi = bfhi(wt[i][e]);
        a2[2 * e] = fmaf(c0, lo, a2[2 * e]); a2[2 * e + 1] = fmaf(c0, hi, a2[2 * e + 1]);
        a2[8 + 2 * e] = fmaf(c1, lo, a2[8 + 2 * e]); a2[9 + 2 * e] = fmaf(c1, hi, a2[9 + 2 * e]);
      }
    }
    float a8[8], a4[4];
#pragma unroll
    for (int e = 0; e < 8; ++e) a8[e] = tr32(a2[e], a2[e + 8]);
#pragma unroll
    for (int e = 0; e < 4; ++e) a4[e] = tr16(a8[e], a8[e + 4]);
    *(LAS f32x2*)(lds + L_XR + w * 128 + hf * 64 + (lane & 7) * 8 + (g4 & 1) * 4 + ((lane >> 3) & 1) * 2) =
        f32x2{trd<XOR8>(a4[0], a4[2], lane & 8), trd<XOR8>(a4[1], a4[3], lane & 8)};
  }
  gbar(ctl, phase, lane);
  v4u axb[2];
#pragma unroll
  for (int ks = 0; ks < 2; ++ks) {
    f32x4 lo = Z4, hi = Z4;
#pragma unroll
    for (int ww = 0; ww < 4; ++ww) {
      const LAS float* sx = lds + L_XR + ww * 128 + ((l15 >> 1) & 1) * 64 + 32 * ks + 8 * g4;
      lo += *(const LAS f32x4*)sx; hi += *(const LAS f32x4*)(sx + 4);
    }
    v4u p = {cvtpk(lo[0], lo[1]), cvtpk(lo[2], lo[3]), cvtpk(hi[0], hi[1]), cvtpk(hi[2], hi[3])};
    if (l15 & 1) p = v4u{cvtpk(lo[0] - bflo(p[0]), lo[1] - bfhi(p[0])), cvtpk(lo[2] - bflo(p[1]), lo[3] - bfhi(p[1])),
                         cvtpk(hi[0] - bflo(p[2]), hi[1] - bfhi(p[2])), cvtpk(hi[2] - bflo(p[3]), hi[3] - bfhi(p[3]))};
    axb[ks] = p;
  }
  u32 yb[2];
  {
    f32x4 d[8];
#pragma unroll
    for (int i = 0; i < 8; ++i) { d[i] = MFMA(axb[0], bfr[i], Z4); d[i] = MFMA(axb[1], bfr[8 + i], d[i]); }
    yb[0] = cvtpk(SEL4(d[0][0] + d[0][1], d[2][0] + d[2][1], d[4][0] + d[4][1], d[6][0] + d[6][1]),
                  SEL4(d[1][0] + d[1][1], d[3][0] + d[3][1], d[5][0] + d[5][1], d[7][0] + d[7][1]));
    yb[1] = cvtpk(SEL4(d[0][2] + d[0][3], d[2][2] + d[2][3], d[4][2] + d[4][3], d[6][2] + d[6][3]),
                  SEL4(d[1][2] + d[1][3], d[3][2] + d[3][3], d[5][2] + d[5][3], d[7][2] + d[7][3]));
  }
  if (w < 3 || l15 < 8) {
    const u32 yo2 = WS_YB + ((h * 16 + r) * 256 + 64 * w + 4 * l15 + g4) * 8;
    st8(rW, yo2, yb[0], tag);
    st8(rW, yo2 + 256 * 256 * 8, yb[1], tag);
  }
  SBAR();
  STAMP(9);
  STAMP(12);
  {
    v4u g[4];
    const u32 so = lane * 16, ss = WS_SA + (bh * 16 + 4 * w) * 1024;
    gather_issue<4, 1024>(g, rW, so, ss);
    gather_wait_slow<4, 1024>(g, rW, so, ss, tag);
    float sc[16];
#pragma unroll
    for (int i = 0; i < 4; ++i) { sc[4 * i] = bflo(g[i].x); sc[4 * i + 1] = bfhi(g[i].x); sc[4 * i + 2] = bflo(g[i].z); sc[4 * i + 3] = bfhi(g[i].z); }
    float m = sc[0];
#pragma unroll
    for (int i = 1; i < 16; ++i) m = fmaxf(m, sc[i]);
    m = mx32(mx16(rowmax16(m))) * 0.0625f;
    float l = 0.f;
#pragma unroll
    for (int i = 0; i < 16; ++i) l += ex2((sc[i] * 0.0625f - m) * LOG2E);
    l = rowsum16(l); l = tr16(l, l); l = tr32(l, l);
    if (lane == 0) { lds[L_YB + w] = m; lds[L_YB + 4 + w] = l; }
    LGKM0();
    if (lane == 0) __hip_atomic_fetch_add(ctl + 6, 1u, __ATOMIC_RELAXED, __HIP_MEMORY_SCOPE_WORKGROUP);
  }
  lds_wait(ctl + 8 + w, 5);
#pragma unroll
  for (int ks = 0; ks < 4; ++ks) {
    v4u t[8];
#pragma unroll
    for (int i = 0; i < 8; ++i) t[i] = *(LAS v4u*)(lds + (ks < 2 ? L_T0 : L_T1) + ((w * 16 + 8 * (ks & 1) + i) * 64 + lane) * 4);
#pragma unroll
    for (int i = 0; i < 8; ++i) { bfr[8 * ks + i] = BFRAG(t, 0, i); asm volatile("" : "+v"(bfr[8 * ks + i])); }
  }
  STAMP(13);
  const rsrc_t rOut = mkrs(out, 2 * 896 * 2);
#define FINAL_SUM(YBASE, COL0, NL, UN, SL) { v4u g[NL]; const int ub = (UN) >= 14, ul = (UN) - 14 * ub; \
    const u32 yo = (8 * (NL) * w + (lane >> 3)) * 2048 + (lane & 7) * 16, ys = (YBASE) + ub * (32 * (NL)) * 2048 + ul * 128; \
    auto ld = [&](int i) { return ld16<AUX_SC1>(rW, yo, ys + i * 8 * 2048); }; gissue<NL>(g, ld); gwait<NL, SL>(g, tag, ld); \
    f32x4 sv = Z4; \
    _Pragma("unroll") for (int i = 0; i < (NL); ++i) sv += f32x4{bflo(g[i].x), bfhi(g[i].x), bflo(g[i].z), bfhi(g[i].z)}; \
    float v = tr16(tr32(sv[0], sv[2]), tr32(sv[1], sv[3])); v += dpp<XOR8>(v); \
    if (!(lane & 8)) lds[L_CW + w * 32 + 4 * (lane & 7) + 2 * hf + (g4 & 1)] = v; \
    gbar(ctl, phase, lane); \
    if (w == 0 && lane < 16) { f32x2 o = {0.f, 0.f}; \
      _Pragma("unroll") for (int ww = 0; ww < 4; ++ww) o += *(LAS f32x2*)(lds + L_CW + ww * 32 + 2 * lane); \
      __builtin_amdgcn_raw_buffer_store_b32(cvtpk(o[0], o[1]), rOut, (ub * 896 + (COL0) + 32 * ul + 2 * lane) * 2, 0, 0); } }
  lds_wait(ctl + 4, 1);
  f32x4 d[8];
#pragma unroll
  for (int i = 0; i < 8; ++i) d[i] = Z4;
#pragma unroll
  for (int ks = 0; ks < 4; ++ks) {
    v4u a = {0u, 0u, 0u, 0u};
    if ((l15 & 2) == 0) a = *(LAS v4u*)(lds + L_OB + (l15 & 1) * 64 + 16 * ks + 4 * g4);
#pragma unroll
    for (int i = 0; i < 8; ++i) d[i] = MFMA(a, bfr[8 * ks + i], d[i]);
  }
  const float y0 = SEL4(d[0][0] + d[0][1], d[2][0] + d[2][1], d[4][0] + d[4][1], d[6][0] + d[6][1]);
  const float y1 = SEL4(d[1][0] + d[1][1], d[3][0] + d[3][1], d[5][0] + d[5][1], d[7][0] + d[7][1]);
  if (w < 3 || l15 < 8) {
    st8(rW, WS_YA + ((vb * 128 + h * 8 + r8) * 256 + 64 * w + 4 * l15 + g4) * 8, cvtpk(y0, y1), tag);
  }
  STAMP(14);
  if (id >= 28 && id < 56) FINAL_SUM(WS_YB, 448, 8, id - 28, TPOLL)
  if (id < 28) {
    FINAL_SUM(WS_YA, 0, 4, id, TPOLL)
    if (tid == 256 && id == 0) __builtin_amdgcn_raw_buffer_store_b32(tag, rW, WS_EPOCH, 0, AUX_SC1);
  }
  STAMP(15);
  STAMP_DUMP();
}

extern "C" void kernel_launch(void* const* d_in, const int* in_sizes, int n_in, void* d_out, int out_size, void* d_ws,
                              size_t ws_size, hipStream_t stream) {
  fused_decode<<<256, 512, 0, stream>>>((const u16*)d_in[0], (const u16*)d_in[1], (const u16*)d_in[2], (const u16*)d_in[3],
                                        (const u16*)d_in[4], (const u16*)d_in[5], (const u16*)d_in[6], (const u16*)d_in[7],
                                        (const u16*)d_in[8], (const u16*)d_in[9], (const u16*)d_in[10], (u16*)d_out,
                                        (unsigned char*)d_ws);
}
```

```cpp
#include <hip/hip_runtime.h>
#include <stdint.h>

typedef unsigned u32;
typedef unsigned short u16;
typedef u32 v4u __attribute__((ext_vector_type(4)));
typedef u32 v2u __attribute__((ext_vector_type(2)));
typedef float f32x4 __attribute__((ext_vector_type(4)));
typedef float f32x2 __attribute__((ext_vector_type(2)));
typedef __bf16 bf16x2 __attribute__((ext_vector_type(2)));
typedef __bf16 bf16x8 __attribute__((ext_vector_type(8)));
typedef __amdgpu_buffer_rsrc_t rsrc_t;
#define LAS __attribute__((address_space(3)))

constexpr int WS_EPOCH = 0;
constexpr int WS_QA = 4096;
constexpr int WS_QB = WS_QA + 131072;
constexpr int WS_SA = WS_QB + 131072;
constexpr int WS_YA = WS_SA + 524288;
constexpr int WS_YB = WS_YA + 524288;
constexpr int WS_QABS = WS_YB + 524288;
constexpr int WS_PB = WS_QABS + 131072;
constexpr int WS_PBS = WS_PB + 524288;
constexpr int WS_CP = WS_PBS + 131072;
constexpr int WS_CPS = WS_CP + 1048576;
constexpr int WS_YS = WS_CPS + 8192;
constexpr int WS_END = WS_YS + 32768;

constexpr int L_Q = 16;
constexpr int L_PBUF = L_Q + 384;
constexpr int L_OB = L_PBUF + 512;
constexpr int L_S3S = L_OB + 512;
constexpr int L_YB = L_S3S + 256;
constexpr int L_S3A = L_YB + 16;
constexpr int L_QRED = L_S3A;
constexpr int L_XR = L_S3A;
constexpr int L_CW = L_S3A + 512;
constexpr int L_T0 = L_S3A + 8 * 16 * 36;
constexpr int L_S3B = L_T0;
constexpr int L_T1 = L_T0 + 16384;
constexpr int L_TOTAL = L_T1 + 16384;
static_assert(L_TOTAL * 4 > 81920 && L_TOTAL * 4 <= 160 * 1024, "lds");

constexpr int AUX_NT = 2, AUX_SC1 = 16;
constexpr float LOG2E = 1.4426950408889634f;
constexpr u32 SPIN_CAP = 1u << 17;


constexpr int DS = 5;
constexpr int NPRE = 6, NPR2 = 16;
constexpr int DA1 = 4, DA = 7, NSW = 48;
constexpr int G_T0 = 640, G_TAU = 285, G_STEP = 50;
constexpr int GPOS = 8;
constexpr int WS1 = 4, WS2 = 66, WS3 = 90;
constexpr int R_A = 240, R_P = 280, R_C = 880;
constexpr int T_A = 920, T_P = 1600, T_C = 2200;
constexpr int QSL2 = 40, QPOLL = 4, POLLS = 10, TPOLL = 4;
constexpr u32 CODE_BYTES = 44672;

__device__ __forceinline__ rsrc_t mkrs(const void* p, u32 bytes) { return __builtin_amdgcn_make_buffer_rsrc((void*)p, 0, bytes, 0x00020000); }
template <int AUX> __device__ __forceinline__ v4u ld16(rsrc_t r, u32 voff, u32 soff) { return __builtin_amdgcn_raw_buffer_load_b128(r, voff, soff, AUX); }
__device__ __forceinline__ void st8(rsrc_t r, u32 off, u32 data, u32 tag) { __builtin_amdgcn_raw_buffer_store_b64(v2u{data, tag}, r, off, 0, AUX_SC1); }
__device__ __forceinline__ void st16(rsrc_t r, u32 off, u32 d0, u32 d1, u32 tag) { __builtin_amdgcn_raw_buffer_store_b128(v4u{d0, tag, d1, tag}, r, off, 0, AUX_SC1); }
__device__ __forceinline__ float asf(u32 x) { return __builtin_bit_cast(float, x); }
__device__ __forceinline__ u32 asu(float x) { return __builtin_bit_cast(u32, x); }
__device__ __forceinline__ float bflo(u32 x) { return asf(x << 16); }
__device__ __forceinline__ float bfhi(u32 x) { return asf(x & 0xffff0000u); }
__device__ __forceinline__ float dot2(u32 a, u32 b, float acc) {
  return __builtin_amdgcn_fdot2_f32_bf16(__builtin_bit_cast(bf16x2, a), __builtin_bit_cast(bf16x2, b), acc, false);
}
__device__ __forceinline__ u32 cvtpk(float lo, float hi) {
  bf16x2 r; r[0] = (__bf16)lo; r[1] = (__bf16)hi; return __builtin_bit_cast(u32, r);
}
__device__ __forceinline__ float ex2(float x) { return __builtin_amdgcn_exp2f(x); }
template <int CTRL> __device__ __forceinline__ float dpp(float x) {
  return asf((u32)__builtin_amdgcn_mov_dpp((int)asu(x), CTRL, 0xf, 0xf, true));
}
constexpr int XOR1 = 0xB1, XOR2 = 0x4E, XOR7 = 0x141, XOR8 = 0x128;
__device__ __forceinline__ float tr16(float a, float b) {
  auto s = __builtin_amdgcn_permlane16_swap(asu(a), asu(b), false, false); return asf(s[0]) + asf(s[1]);
}
__device__ __forceinline__ float tr32(float a, float b) {
  auto s = __builtin_amdgcn_permlane32_swap(asu(a), asu(b), false, false); return asf(s[0]) + asf(s[1]);
}
__device__ __forceinline__ float mx16(float a) { auto s = __builtin_amdgcn_permlane16_swap(asu(a), asu(a), false, false); return fmaxf(asf(s[0]), asf(s[1])); }
__device__ __forceinline__ float mx32(float a) { auto s = __builtin_amdgcn_permlane32_swap(asu(a), asu(a), false, false); return fmaxf(asf(s[0]), asf(s[1])); }
template <int CTRL> __device__ __forceinline__ float trd(float lo, float hi, bool bit) {
  const float keep = bit ? hi : lo, send = bit ? lo : hi; return keep + dpp<CTRL>(send);
}
__device__ __forceinline__ float rowsum16(float x) { x += dpp<XOR1>(x); x += dpp<XOR2>(x); x += dpp<XOR7>(x); x += dpp<XOR8>(x); return x; }
__device__ __forceinline__ float rowmax16(float x) { x = fmaxf(x, dpp<XOR1>(x)); x = fmaxf(x, dpp<XOR2>(x)); x = fmaxf(x, dpp<XOR7>(x)); x = fmaxf(x, dpp<XOR8>(x)); return x; }
__device__ __forceinline__ float red16x32(const float (&x)[16], int lane) {
  float y[8], z[4], u[2];
#pragma unroll
  for (int i = 0; i < 8; ++i) y[i] = tr16(x[i], x[i + 8]);
#pragma unroll
  for (int i = 0; i < 4; ++i) z[i] = trd<XOR8>(y[i], y[i + 4], lane & 8);
#pragma unroll
  for (int i = 0; i < 2; ++i) u[i] = trd<XOR7>(z[i], z[i + 2], lane & 4);
  float v = trd<XOR2>(u[0], u[1], lane & 2);
  return v + dpp<XOR1>(v);
}
__device__ __forceinline__ bool tags_ok(v4u g, u32 tag) { return g.y == tag && g.w == tag; }
__device__ __forceinline__ bool all_ok(bool ok) { return __builtin_amdgcn_ballot_w64(!ok) == 0ull; }
#define CBAR() asm volatile("" ::: "memory")
#define SBAR() __builtin_amdgcn_sched_barrier(0)
#define LGKM0() asm volatile("s_waitcnt lgkmcnt(0)" ::: "memory")

__device__ __forceinline__ void gbar(LAS u32* ctr, u32& phase, int lane) {
  phase += 4;
  LGKM0();
  if (lane == 0) __hip_atomic_fetch_add(ctr, 1u, __ATOMIC_RELAXED, __HIP_MEMORY_SCOPE_WORKGROUP);
#pragma unroll 1
  for (u32 s = 0; s < (1u << 24); ++s) {
    const u32 v = __builtin_amdgcn_readfirstlane(__hip_atomic_load(ctr, __ATOMIC_RELAXED, __HIP_MEMORY_SCOPE_WORKGROUP));
    if (v >= phase) break;
  }
  CBAR();
}
template <int N, int SL = POLLS, class F> __device__ __forceinline__ void gwait(v4u (&g)[N], u32 tag, F&& ld) {
  bool ok = true;
#pragma unroll
  for (int i = 0; i < N; ++i) ok = ok && tags_ok(g[i], tag);
  if (!all_ok(ok)) {
#pragma unroll 1
    for (u32 s = 0; s < SPIN_CAP; ++s) {
      __builtin_amdgcn_s_sleep(SL);
      CBAR();
      if (!ok) {
#pragma unroll
        for (int i = 0; i < N; ++i) g[i] = ld(i);
      }
      ok = true;
#pragma unroll
      for (int i = 0; i < N; ++i) ok = ok && tags_ok(g[i], tag);
      if (all_ok(ok)) break;
    }
  }
}
template <int N, class F> __device__ __forceinline__ void gissue(v4u (&g)[N], F&& ld) {
#pragma unroll
  for (int i = 0; i < N; ++i) g[i] = ld(i);
}
__device__ __forceinline__ void slot(unsigned long long t0, u32 ticks) {
#pragma unroll 1
  while ((u32)(__builtin_amdgcn_s_memrealtime() - t0) < ticks) __builtin_amdgcn_s_sleep(2);
}
__device__ __forceinline__ void lds_wait(LAS u32* flag, u32 want) {
#pragma unroll 1
  for (u32 s = 0; s < (1u << 24); ++s) {
    if ((u32)__builtin_amdgcn_readfirstlane(__hip_atomic_load(flag, __ATOMIC_RELAXED, __HIP_MEMORY_SCOPE_WORKGROUP)) >= want) break;
    __builtin_amdgcn_s_sleep(1);
  }
  CBAR();
}
__host__ __device__ constexpr bool sideon(int n) { return (n > WS1 + 1 && n < WS1 + 18) || (n > WS2 + 1 && n < WS2 + 18) || (n > WS3 + 1 && n < WS3 + 18); }
__host__ __device__ constexpr int adist(int n) { return n < NSW ? DA1 : DA; }
template <int N> __device__ __forceinline__ void vmw() { asm volatile("s_waitcnt vmcnt(%0)" :: "i"(N) : "memory"); }
#define WGBAR() { LGKM0(); __builtin_amdgcn_s_barrier(); CBAR(); }
#define MFMA(A, B, C) __builtin_amdgcn_mfma_f32_16x16x32_bf16(__builtin_bit_cast(bf16x8, A), __builtin_bit_cast(bf16x8, B), C, 0, 0, 0)
#define PSEL(i) (((i) & 1) ? 0x07060302u : 0x05040100u)
#define BFRAG(T, O, i) v4u{__builtin_amdgcn_perm(T[(O) + 1][(i) >> 1], T[(O)][(i) >> 1], PSEL(i)), __builtin_amdgcn_perm(T[(O) + 3][(i) >> 1], T[(O) + 2][(i) >> 1], PSEL(i)), \
                           __builtin_amdgcn_perm(T[(O) + 5][(i) >> 1], T[(O) + 4][(i) >> 1], PSEL(i)), __builtin_amdgcn_perm(T[(O) + 7][(i) >> 1], T[(O) + 6][(i) >> 1], PSEL(i))}
#define SEL4(A0, A1, A2, A3) (g4 == 0 ? (A0) : g4 == 1 ? (A1) : g4 == 2 ? (A2) : (A3))
constexpr f32x4 Z4 = {0.f, 0.f, 0.f, 0.f};

#define QSLICE() { float a[16]; \
    _Pragma("unroll") for (int e = 0; e < 16; ++e) a[e] = 0.f; \
    _Pragma("unroll") for (int i = 0; i < 7; ++i) { \
      asm volatile("" : "+v"(wqv[i]), "+v"(xv[0][i]), "+v"(xv[1][i])); \
      const float x0 = bflo(xv[0][i]), x1 = bflo(xv[1][i]); \
      _Pragma("unroll") for (int e = 0; e < 4; ++e) { const float lo = bflo(wqv[i][e]), hi = bfhi(wqv[i][e]); \
        a[2 * e] = fmaf(x0, lo, a[2 * e]); a[2 * e + 1] = fmaf(x0, hi, a[2 * e + 1]); \
        a[8 + 2 * e] = fmaf(x1, lo, a[8 + 2 * e]); a[9 + 2 * e] = fmaf(x1, hi, a[9 + 2 * e]); } } \
    float a8[8], a4[4]; \
    _Pragma("unroll") for (int e = 0; e < 8; ++e) a8[e] = tr32(a[e], a[e + 8]);              \
    _Pragma("unroll") for (int e = 0; e < 4; ++e) a4[e] = tr16(a8[e], a8[e + 4]);            \
    const float c0 = trd<XOR8>(a4[0], a4[2], lane & 8), c1 = trd<XOR8>(a4[1], a4[3], lane & 8);     \
    *(LAS f32x2*)(lds + L_QRED + w * 128 + hf * 64 + (lane & 7) * 8 + (g4 & 1) * 4 + ((lane >> 3) & 1) * 2) = f32x2{c0, c1}; \
    gbar(ctl, phase, lane); \
    if (w == 0) { f32x2 s = {0.f, 0.f}; \
      _Pragma("unroll") for (int ww = 0; ww < 4; ++ww) s += *(LAS f32x2*)(lds + L_QRED + ww * 128 + 2 * lane); \
      st16(rW, (isB ? WS_QB : WS_QA) + ((((h * 2 + hf) * 2 + qk) * 256) + qb * 64 + 2 * rl) * 8, asu(s[0]), asu(s[1]), tag); } }
#define QGATHER(BASE, BB, LQ, FLAG) { v4u g[4]; const u32 qo = lane * 16, qs = (BASE) + (h * 2 + (BB)) * 4096; \
    auto ld = [&](int i) { return ld16<AUX_SC1>(rW, qo, qs + i * 1024); }; gissue<4>(g, ld); gwait<4, QPOLL>(g, tag, ld); \
    ((LAS u32*)(lds + L_Q))[(LQ) + lane] = cvtpk(asf(g[0].x) + asf(g[2].x), asf(g[0].z) + asf(g[2].z)); \
    ((LAS u32*)(lds + L_Q))[(LQ) + 64 + lane] = cvtpk(asf(g[1].x) + asf(g[3].x), asf(g[1].z) + asf(g[3].z)); \
    LGKM0(); \
    if (lane == 0) __hip_atomic_fetch_add(ctl + (FLAG), 1u, __ATOMIC_RELAXED, __HIP_MEMORY_SCOPE_WORKGROUP); }
#define KDOT(s) { float d = dot2(c.x, qp[0], 0.f); d = dot2(c.y, qp[1], d); d = dot2(c.z, qp[2], d); xs[s] = dot2(c.w, qp[3], d); \
    asm volatile("" : "+v"(xs[s])); SBAR(); }
#define PUBLISH_SCORES(SG, SA_, SB_) { const int idx = (lane >> 1) & 15; \
    if (!(lane & 1)) st8(rW, WS_SA + (((bh * 16 + (SG)) * 4 + w) * 32 + (idx & 1) * 16 + hf * 8 + (idx >> 1)) * 8, cvtpk(SA_, SB_), tag); }
__global__ void __launch_bounds__(512, 2)
fused_decode(const u16* __restrict__ xA, const u16* __restrict__ Kc, const u16* __restrict__ Vc, const u16* __restrict__ wqA,
             const u16* __restrict__ woA, const u16* __restrict__ xB, const u16* __restrict__ ckv, const u16* __restrict__ wqB,
             const u16* __restrict__ wuk, const u16* __restrict__ wuv, const u16* __restrict__ woB, u16* out,
             unsigned char* ws) {
  unsigned long long pc0;
  asm volatile("s_getpc_b64 %0" : "=s"(pc0));
  pc0 &= ~255ull;
  __shared__ __attribute__((aligned(16))) float lds_[L_TOTAL];
  LAS float* lds = (LAS float*)lds_;
  LAS u32* ctl = (LAS u32*)lds_;
  const int tid = threadIdx.x, lane = tid & 63;
  const int wave = __builtin_amdgcn_readfirstlane(tid >> 6), hw = wave >> 2, w = wave & 3;
  const int id = blockIdx.x, x = id & 7, y = id >> 3;
  const int h = x + 8 * (y >> 4), r = y & 15, r8 = r & 7;
  const bool isB = r < 8;
  const int hf = lane >> 5, rl = lane & 31, l15 = lane & 15, g4 = lane >> 4;
  const int vb = r >> 3, dh = r8 >> 2, vu = r8 & 3, bh = vb * 16 + h;
  const int dq = (r8 >> 1) ^ 2, ch = r8 & 1;
  const int qb = 3 - (r8 >> 1), qk = r8 & 1;
  const int grp = x * 2 + (y >> 4), ab = grp >> 3, kg = grp & 7, kb = 16 * kg + r;
  const rsrc_t rW = mkrs(ws, WS_END);
  const unsigned long long t0 = __builtin_amdgcn_s_memrealtime();
  const rsrc_t rC = mkrs(ckv, 2 * 4096 * 1024 * 2);
  const u32 avo = (l15 & 7) * 2048 + w * 512 + (l15 >> 3) * 64 + g4 * 16, as0 = (u32)(ab * 4096 + 32 * kb) * 2048;
  const u32 ovo = (w == 3 && l15 >= 8) ? 0x7fffff00u : (u32)(8 * g4 * 896 + w * 256 + l15 * 16);
  const rsrc_t r2 = isB ? rC : mkrs(woB, 4096 * 448 * 2);
  const u32 bvo = isB ? (u32)((128 * w + 8 * g4) * 2048 + l15 * 16) : ovo, bst = isB ? 2048u : 896u;
  const u32 bs0 = isB ? (u32)(ab * 4096 + 512 * kg) * 2048 + r8 * 256 : (u32)(h * 256 + 64 * dq) * 896;
#define TISSUE(m) { if ((m) < 16) sd[(m) % DS] = ld16<0>(rC, avo + ((m) & 3) * 128, as0 + ((m) >> 2) * 8 * 2048); \
    else if ((m) < 32) sd[(m) % DS] = ld16<0>(r2, bvo, bs0 + ((((m) >> 3) & 1) * 32 + ((m) & 7)) * bst); }
  const int cr = r;
  v4u sd[DS], wqv[7], wt[8]; u32 xv[2][7];
  if (hw == 0) {
    u32 ep = __builtin_amdgcn_raw_buffer_load_b32(rW, WS_EPOCH, 0, AUX_SC1);
    const v4u ci = ld16<0>(mkrs((const void*)pc0, CODE_BYTES), lane * 16, (y * 2 + (w & 1)) * 1024 + (w >> 1) * 0x10000000);
    SBAR();
#pragma unroll
    for (int m = 0; m < DS; ++m) TISSUE(m)
    SBAR();
    asm volatile("" : "+v"(ep) : "v"(ci));
    if (tid < 12) ctl[tid] = tid == 2 ? ep + 1u : 0u;
  } else {
    const rsrc_t rX = mkrs(isB ? xB : xA, 2 * 448 * 2);
    const rsrc_t rWq = mkrs(isB ? wqB : wqA, 448 * 4096 * 2), rUK = mkrs(wuk, 1024 * 4096 * 2);
    const int row0 = 224 * qk + 7 * (8 * w + (lane >> 3));
#pragma unroll
    for (int i = 0; i < 7; ++i) wqv[i] = ld16<AUX_NT>(rWq, row0 * 8192 + (lane & 7) * 16, i * 8192 + (h * 256 + qb * 64) * 2);
#pragma unroll
    for (int i = 0; i < 7; ++i) {
      xv[0][i] = __builtin_amdgcn_raw_buffer_load_b16(rX, row0 * 2 + i * 2, 0, 0);
      xv[1][i] = __builtin_amdgcn_raw_buffer_load_b16(rX, row0 * 2 + i * 2, 896, 0);
    }
    SBAR();
#pragma unroll
    for (int i = 0; i < 8; ++i) wt[i] = ld16<AUX_NT>(rUK, (16 * w + 8 * hf) * 8192 + rl * 16, (64 * cr + i) * 8192 + h * 512);
    SBAR();
  }
  WGBAR();
  const u32 tag = __builtin_amdgcn_readfirstlane(ctl[2]);
  u32 phase = 0;

  if (hw == 0) {
    const rsrc_t rK = mkrs(Kc, 64u << 20), rV = mkrs(Vc, 64u << 20);
    const u32 kvo = w * 1024 + lane * 16, vvo = (4 * w + g4) * 512 + l15 * 16;
    const u32 ks0 = (u32)(bh * 4096 + 256 * r8) * 512;
    const u32 vs0 = (u32)(bh * 4096 + 512 * vu) * 512 + dh * 256;
    v4u buf[16];
#define ISSUE(m) { if ((m) < 64) buf[(m) & 15] = ld16<AUX_NT>(rK, kvo, ks0 + ((m) >> 5) * (2048 * 512) + ((m) & 31) * 4096); \
      else if ((m) < 128) buf[(m) & 15] = ld16<AUX_NT>(rV, vvo, vs0 + (((m) >> 5) & 1) * (2048 * 512) + ((m) & 31) * 8192); }
    {
#pragma unroll
      for (int m = 0; m < 32; ++m) {
        const v4u c = sd[m % DS];
        TISSUE(m + DS)
        if (m >= 32 - NPRE) ISSUE(m - (32 - NPRE))
        *(LAS v4u*)(lds + (m < 16 ? L_T0 : L_T1) + ((w * 16 + (m & 15)) * 64 + lane) * 4) = c;
        if (m == 15 || m == 31) { LGKM0(); if (lane == 0) __hip_atomic_store(ctl + 8 + w, (u32)(m >> 4) + 1u, __ATOMIC_RELAXED, __HIP_MEMORY_SCOPE_WORKGROUP); }
        SBAR();
      }
    }
#pragma unroll
    for (int s = NPRE; s < NPR2; ++s) { vmw<DA1>(); ISSUE(s) SBAR(); }
    lds_wait(ctl + 3, 1);
    const v4u qp = *(LAS v4u*)(lds + L_Q + 4 * rl);
    int gmode = 1;
#define GOVERN(n) { if ((n) >= NSW && !((n) & 1) && (n) < 128 - DA) { \
        if (((n) & 7) == 0) { const int lag = (int)(u32)(__builtin_amdgcn_s_memrealtime() - t0) - (G_T0 + (((n) * G_TAU) >> 4)); \
          gmode = lag < -G_STEP ? 0 : lag < 0 ? 1 : lag < G_STEP ? 2 : 3; } \
        if (sideon(n)) { if (gmode == 0) vmw<2 * (DA - 4) - 1>(); else if (gmode == 1) vmw<2 * (DA - 3) - 1>(); else if (gmode == 2) vmw<2 * (DA - 2) - 1>(); } \
        else { if (gmode == 0) vmw<DA - 4>(); else if (gmode == 1) vmw<DA - 3>(); else if (gmode == 2) vmw<DA - 2>(); } } }
#define REFILL(n) { GOVERN(n) if ((n) < DA1) { ISSUE((n) + NPR2) } else if ((n) >= NPR2) { \
      _Pragma("unroll") for (int m_ = (n) + adist((n) - 1); m_ <= (n) + adist(n); ++m_) { ISSUE(m_) } } }
#define PPOS(i) (16 * ((i) >> 4) + 2 * ((i) & 7) + (((i) >> 3) & 1))
    const u32 go_s = WS_SA + (((bh * 16 + 2 * vu + ((rl >> 2) & 1)) * 4 + 2 * (w & 1) + (rl >> 4)) * 32 + (w >> 1) * 16 + ((rl >> 3) & 1) * 8 + 2 * (rl & 3)) * 8;
    const rsrc_t rOA = mkrs(woA, 4096 * 448 * 2);
    const u32 woa = (u32)(h * 256 + 128 * dh) * 896;
    const rsrc_t rj1 = isB ? rC : rOA, rj3 = mkrs(woA, isB ? 4096 * 448 * 2 : 0);
    const u32 sj1 = isB ? bs0 + 64 * 2048 : woa, sj2 = isB ? woa : woa + 64 * 896;
    const int dj2 = isB ? L_T1 : L_T0;
    v4u sw[8];
#define SIDE1(n, S, RS, VO, SO, ST, DST, NEED, DONE, WR) { \
      if ((n) >= (S) && (n) < (S) + 16) { const int j_ = (n) - (S); sw[(n) & 7] = ld16<0>(RS, VO, (SO) + (32 * (j_ >> 3) + (j_ & 7)) * (ST)); } \
      if ((n) >= (S) + 7 && (n) < (S) + 23) { const int j_ = (n) - (S) - 7; \
        if (j_ == 0) lds_wait(ctl + 7, NEED); \
        if (WR) *(LAS v4u*)(lds + (DST) + ((w * 16 + j_) * 64 + lane) * 4) = sw[((n) - 7) & 7]; \
        if (j_ == 15) { LGKM0(); if (lane == 0) __hip_atomic_store(ctl + 8 + w, DONE, __ATOMIC_RELAXED, __HIP_MEMORY_SCOPE_WORKGROUP); } } }
#define SIDE(n) { SIDE1(n, WS1, rj1, bvo, sj1, bst, L_T1, 4, 3u, true) SIDE1(n, WS2, rOA, ovo, sj2, 896, dj2, 8, 4u, true) \
      SIDE1(n, WS3, rj3, ovo, woa + 64 * 896, 896, L_T0, isB ? 12 : 8, 5u, isB) }
    v4u gs[1];
    {
      float xs[16], sa, sb;
#pragma unroll
      for (int sl = 0; sl < 2; ++sl) {
#pragma unroll
        for (int s = 0; s < 16; ++s) { const v4u c = buf[s]; REFILL(32 * sl + s) SIDE(32 * sl + s) KDOT(s) }
        sa = red16x32(xs, lane);
#pragma unroll
        for (int s = 0; s < 16; ++s) {
          if (sl == 1 && s == 16 - GPOS) { gs[0] = ld16<AUX_SC1>(rW, go_s, 0); SBAR(); }
          const v4u c = buf[s]; REFILL(32 * sl + 16 + s) SIDE(32 * sl + 16 + s) KDOT(s)
        }
        sb = red16x32(xs, lane);
        PUBLISH_SCORES(8 * sl + r8, sa, sb)
      }
    }
    LAS float* pb = lds + L_PBUF + w * 128;
    float acc[8], pv[32], mrun = -INFINITY, lrun = 0.f;
#pragma unroll
    for (int e = 0; e < 8; ++e) acc[e] = 0.f;
#define MAKE_P() { \
    const float s0 = bflo(gs[0].x) * 0.0625f, s1 = bfhi(gs[0].x) * 0.0625f, s2 = bflo(gs[0].z) * 0.0625f, s3 = bfhi(gs[0].z) * 0.0625f; \
    const float mn = fmaxf(mrun, mx16(rowmax16(fmaxf(fmaxf(s0, s1), fmaxf(s2, s3))))); \
    const float sc = ex2((mrun - mn) * LOG2E); mrun = mn; \
    _Pragma("unroll") for (int e = 0; e < 8; ++e) acc[e] *= sc; \
    const f32x4 p4 = {ex2((s0 - mn) * LOG2E), ex2((s1 - mn) * LOG2E), ex2((s2 - mn) * LOG2E), ex2((s3 - mn) * LOG2E)}; \
    lrun = fmaf(lrun, sc, (p4[0] + p4[1]) + (p4[2] + p4[3])); \
    if (lane < 32) *(LAS f32x4*)(pb + lane * 4) = p4; \
    LGKM0(); \
    _Pragma("unroll") for (int u = 0; u < 8; ++u) { const f32x4 t = *(LAS f32x4*)(pb + g4 * 32 + u * 4); \
      pv[4 * u] = t[0]; pv[4 * u + 1] = t[1]; pv[4 * u + 2] = t[2]; pv[4 * u + 3] = t[3]; } \
    LGKM0(); }
#define VSTEPS(N0, S0, S1) _Pragma("unroll") for (int s = (S0); s < (S1); ++s) { const v4u c = buf[s & 15]; REFILL((N0) + s) SIDE((N0) + s) \
      const float p = pv[PPOS(s)]; \
      _Pragma("unroll") for (int e = 0; e < 4; ++e) { acc[2 * e] = fmaf(p, bflo(c[e]), acc[2 * e]); acc[2 * e + 1] = fmaf(p, bfhi(c[e]), acc[2 * e + 1]); } \
      asm volatile("" : "+v"(acc[0]), "+v"(acc[1]), "+v"(acc[2]), "+v"(acc[3]), "+v"(acc[4]), "+v"(acc[5]), "+v"(acc[6]), "+v"(acc[7])); SBAR(); }
    gwait<1>(gs, tag, [&](int) { return ld16<AUX_SC1>(rW, go_s, 0); });
    MAKE_P()
    VSTEPS(64, 0, 32 - GPOS)
    gs[0] = ld16<AUX_SC1>(rW, go_s, 8192);
    SBAR();
    VSTEPS(64, 32 - GPOS, 32)
    gwait<1>(gs, tag, [&](int) { return ld16<AUX_SC1>(rW, go_s, 8192); });
    MAKE_P()
    {
      float lw = rowsum16(lrun); lw = tr16(lw, lw);
      if (lane == 0) { lds[L_YB + w] = mrun; lds[L_YB + 4 + w] = lw; }
      LGKM0();
      u32 o6 = 0;
      if (lane == 0) o6 = __hip_atomic_fetch_add(ctl + 6, 1u, __ATOMIC_RELAXED, __HIP_MEMORY_SCOPE_WORKGROUP);
      o6 = __builtin_amdgcn_readfirstlane(o6);
      CBAR();
      if (o6 == 3) {
        const f32x4 m4 = *(LAS f32x4*)(lds + L_YB), l4 = *(LAS f32x4*)(lds + L_YB + 4);
        const float M = fmaxf(fmaxf(m4[0], m4[1]), fmaxf(m4[2], m4[3]));
        const float L = (l4[0] * ex2((m4[0] - M) * LOG2E) + l4[1] * ex2((m4[1] - M) * LOG2E)) + (l4[2] * ex2((m4[2] - M) * LOG2E) + l4[3] * ex2((m4[3] - M) * LOG2E));
        if (lane == 0) st16(rW, WS_YS + (vb * 128 + h * 8 + r8) * 128, asu(M), asu(L), tag);
      }
    }
    VSTEPS(96, 0, 32)
    lds_wait(ctl + 6, 4);
    float fin;
    {
      const f32x4 m4 = *(LAS f32x4*)(lds + L_YB);
      fin = ex2((mrun - fmaxf(fmaxf(m4[0], m4[1]), fmaxf(m4[2], m4[3]))) * LOG2E);
    }
    float r4[4];
#pragma unroll
    for (int e = 0; e < 4; ++e) r4[e] = tr32(acc[e], acc[e + 4]);
    const float r0 = tr16(r4[0], r4[2]) * fin, r1 = tr16(r4[1], r4[3]) * fin;
    const u32 hi = cvtpk(r0, r1);
    ((LAS u32*)(lds + L_OB))[w * 64 + 4 * l15 + 2 * hf + (g4 & 1)] = hi;
    ((LAS u32*)(lds + L_OB))[(4 + w) * 64 + 4 * l15 + 2 * hf + (g4 & 1)] = cvtpk(r0 - bflo(hi), r1 - bfhi(hi));
    LGKM0();
    if (lane == 0) __hip_atomic_fetch_add(ctl + 4, 1u, __ATOMIC_RELAXED, __HIP_MEMORY_SCOPE_WORKGROUP);
    return;
  }

  const rsrc_t rUV = mkrs(wuv, 1024 * 4096 * 2);
  v4u ta[16];
  QSLICE()
  __builtin_amdgcn_s_sleep(QSL2);
  if (w == 3) QGATHER(WS_QA, vb, 0, 3)
  if (w == 1 || w == 2) QGATHER(WS_QB, w - 1, 128 * w, 5)
  lds_wait(ctl + 5, 2);
  {
    const v4u q0 = *(LAS v4u*)(lds + L_Q + 128 + 4 * rl), q1 = *(LAS v4u*)(lds + L_Q + 256 + 4 * rl);
    float xs[16];
#pragma unroll
    for (int t = 0; t < 8; ++t) {
      float d = dot2(wt[t].x, q0.x, 0.f); d = dot2(wt[t].y, q0.y, d); d = dot2(wt[t].z, q0.z, d); xs[t] = dot2(wt[t].w, q0.w, d);
      d = dot2(wt[t].x, q1.x, 0.f); d = dot2(wt[t].y, q1.y, d); d = dot2(wt[t].z, q1.z, d); xs[8 + t] = dot2(wt[t].w, q1.w, d);
    }
    const float rr = red16x32(xs, lane);
    const float rn = dpp<XOR2>(rr);
    const int idx = (lane >> 1) & 15;
    if ((lane & 3) == 0) ((LAS u32*)(lds + L_S3S))[(idx >> 3) * 32 + 8 * w + 4 * hf + ((idx & 7) >> 1)] = cvtpk(rr, rn);
  }
  gbar(ctl, phase, lane);
  if (w == 0) st8(rW, WS_QABS + ((hf * 16 + h) * 512 + 32 * cr + rl) * 8, ((LAS u32*)(lds + L_S3S))[lane], tag);
  SBAR();
  u32 tp = (u32)(__builtin_amdgcn_s_memrealtime() - t0);
  v4u bfr[32];
#define PULL(SRC, O) { _Pragma("unroll") for (int ks = 0; ks < 2; ++ks) { v4u t[8]; \
      _Pragma("unroll") for (int i = 0; i < 8; ++i) t[i] = *(LAS v4u*)(lds + (SRC) + ((w * 16 + 8 * ks + i) * 64 + lane) * 4); \
      _Pragma("unroll") for (int i = 0; i < 8; ++i) { bfr[(O) + 8 * ks + i] = BFRAG(t, 0, i); asm volatile("" : "+v"(bfr[(O) + 8 * ks + i])); } } \
    LGKM0(); if (lane == 0) __hip_atomic_fetch_add(ctl + 7, 1u, __ATOMIC_RELAXED, __HIP_MEMORY_SCOPE_WORKGROUP); }
  {
    v4u g[16];
    const u32 qo = lane * 16, qs = WS_QABS + ab * 65536 + w * 1024;
    lds_wait(ctl + 8 + w, 2);
    PULL(L_T1, 0)
    slot(t0, max((u32)T_A, tp + R_A));
    auto ld = [&](int i) { return ld16<AUX_SC1>(rW, qo, qs + i * 4096); };
    gissue<16>(g, ld);
#pragma unroll
    for (int i = 0; i < 16; ++i) ta[i] = *(LAS v4u*)(lds + L_T0 + ((w * 16 + i) * 64 + lane) * 4);
    gwait<16>(g, tag, ld);
    LAS u32* qt = (LAS u32*)(lds + L_T0 + w * 4096);
    LGKM0();
#pragma unroll
    for (int i = 0; i < 16; ++i) *(LAS v2u*)(qt + i * 132 + 2 * lane) = v2u{g[i].x, g[i].z};
    LGKM0();
    v4u af[8];
#pragma unroll
    for (int ks = 0; ks < 8; ++ks) af[ks] = *(LAS v4u*)(qt + l15 * 132 + 16 * ks + 4 * g4);
    f32x4 s1[4] = {Z4, Z4, Z4, Z4}, s2[4] = {Z4, Z4, Z4, Z4};
#pragma unroll
    for (int ln = 0; ln < 4; ++ln) {
      const v4u a1 = af[2 * ln], a2 = af[2 * ln + 1];
#pragma unroll
      for (int rb = 0; rb < 4; ++rb) { s1[rb] = MFMA(a1, ta[4 * rb + ln], s1[rb]); s2[rb] = MFMA(a2, ta[4 * rb + ln], s2[rb]); }
    }
#pragma unroll
    for (int rb = 0; rb < 4; ++rb)
#pragma unroll
      for (int q = 0; q < 4; ++q)
        lds[L_S3A + ((2 * w + (l15 >> 3)) * 16 + 4 * g4 + q) * 36 + 8 * rb + (l15 & 7)] = (l15 & 8) ? s2[rb][q] : s1[rb][q];
    gbar(ctl, phase, lane);
    const int hh = 4 * w + g4;
    f32x2 sv = {0.f, 0.f};
#pragma unroll
    for (int ww = 0; ww < 8; ++ww) sv += *(LAS f32x2*)(lds + L_S3A + (ww * 16 + hh) * 36 + 2 * l15);
    sv *= 0.0625f;
    const float m = rowmax16(fmaxf(sv[0], sv[1]));
    const u32 pk = cvtpk(ex2((sv[0] - m) * LOG2E), ex2((sv[1] - m) * LOG2E));
    const float l = rowsum16(bflo(pk) + bfhi(pk));
    st8(rW, WS_PB + (((ab * 128 + kb) * 16 + hh) * 16 + l15) * 8, pk, tag);
    if (l15 == 0) st16(rW, WS_PBS + ((ab * 128 + kb) * 4 + w) * 128 + g4 * 16, asu(m), asu(l), tag);
    SBAR();
  }
  gbar(ctl, phase, lane);
  tp = (u32)(__builtin_amdgcn_s_memrealtime() - t0);
  u32 yb0 = 0, yb1 = 0;
  if (isB) {
    v4u g[12];
    const u32 ss = WS_PB + (ab * 128 + 16 * kg + 4 * w) * 2048;
    const u32 to = g4 * 512 + (l15 >> 2) * 128 + (l15 & 3) * 16, ts = WS_PBS + (ab * 128 + 16 * kg) * 512;
    lds_wait(ctl + 8 + w, 3);
    PULL(L_T1, 16)
    slot(t0, max((u32)T_P, tp + R_P));
    auto ld = [&](int i) { return i < 8 ? ld16<AUX_SC1>(rW, lane * 16, ss + i * 1024) : ld16<AUX_SC1>(rW, to, ts + (i & 3) * 2048); };
    gissue<12>(g, ld);
    gwait<12>(g, tag, ld);
    LAS u32* pt = (LAS u32*)(lds + L_T0 + 9216 + w * 1280);
#pragma unroll
    for (int i = 0; i < 8; ++i) *(LAS v2u*)(pt + (8 * i + (lane >> 3)) * 20 + 2 * (lane & 7)) = v2u{g[i].x, g[i].z};
    LGKM0();
    v4u pr[4];
#pragma unroll
    for (int pm = 0; pm < 4; ++pm) pr[pm] = *(LAS v4u*)(pt + (16 * pm + l15) * 20 + 4 * g4);
    const float M = mx32(mx16(fmaxf(fmaxf(asf(g[8].x), asf(g[9].x)), fmaxf(asf(g[10].x), asf(g[11].x)))));
    float ek[4];
#pragma unroll
    for (int k = 0; k < 4; ++k) ek[k] = ex2((asf(g[8 + k].x) - M) * LOG2E);
    const float ew = w == 0 ? ek[0] : w == 1 ? ek[1] : w == 2 ? ek[2] : ek[3];
    f32x4 ca[8];
#pragma unroll
    for (int pm = 0; pm < 4; ++pm) {
      const float sc = __shfl(ew, pm * 16 + l15);
      const v4u pf = {cvtpk(sc * bflo(pr[pm][0]), sc * bfhi(pr[pm][0])), cvtpk(sc * bflo(pr[pm][1]), sc * bfhi(pr[pm][1])),
                      cvtpk(sc * bflo(pr[pm][2]), sc * bfhi(pr[pm][2])), cvtpk(sc * bflo(pr[pm][3]), sc * bfhi(pr[pm][3]))};
#pragma unroll
      for (int i = 0; i < 8; ++i) ca[i] = MFMA(pf, bfr[8 * pm + i], pm ? ca[i] : Z4);
    }
#pragma unroll
    for (int q = 0; q < 4; ++q) {
      LAS float* d = lds + L_S3B + (w * 16 + 4 * g4 + q) * 132 + 8 * l15;
      *(LAS f32x4*)d = f32x4{ca[0][q], ca[1][q], ca[2][q], ca[3][q]};
      *(LAS f32x4*)(d + 4) = f32x4{ca[4][q], ca[5][q], ca[6][q], ca[7][q]};
    }
    if (r == 0 && w == 3) {
      float L = (ek[0] * asf(g[8].z) + ek[1] * asf(g[9].z)) + (ek[2] * asf(g[10].z) + ek[3] * asf(g[11].z)); L = tr16(L, L); L = tr32(L, L);
      if (lane < 16) st16(rW, WS_CPS + ((ab * 8 + kg) * 16 + lane) * 16, asu(M), asu(L), tag);
    }
    gbar(ctl, phase, lane);
#pragma unroll
    for (int i = 0; i < 4; ++i) {
      const int hh = w + 4 * i;
      f32x2 vv = {0.f, 0.f};
#pragma unroll
      for (int ww = 0; ww < 4; ++ww) vv += *(LAS f32x2*)(lds + L_S3B + (ww * 16 + hh) * 132 + 2 * lane);
      st8(rW, WS_CP + ((((ab * 8 + kg) * 8 + r8) * 16 + hh) * 64 + lane) * 8, cvtpk(vv[0], vv[1]), tag);
    }
    SBAR();
    gbar(ctl, phase, lane);
    if (lane == 0) __hip_atomic_fetch_add(ctl + 7, 1u, __ATOMIC_RELAXED, __HIP_MEMORY_SCOPE_WORKGROUP);
  } else {
    if (lane == 0) __hip_atomic_fetch_add(ctl + 7, 1u, __ATOMIC_RELAXED, __HIP_MEMORY_SCOPE_WORKGROUP);
    v4u wv[16];
#pragma unroll
    for (int i = 0; i < 16; ++i) {
      vmw<3>();
      wv[i] = ld16<AUX_NT>(rUV, (128 * w + (lane >> 3)) * 8192 + (lane & 7) * 16, (512 * ch + 8 * i) * 8192 + h * 512 + dq * 128);
      SBAR();
    }
    v4u g0[9];
    const u32 co = hf * (8 * 16 * 512) + rl * 16, cs = WS_CP + ((4 * ch + w) * 16 + h) * 512;
    const u32 to = l15 * 256, ts = WS_CPS + h * 16;
    slot(t0, max((u32)T_C, tp + R_C));
    auto ld = [&](int i) { return i < 8 ? ld16<AUX_SC1>(rW, co, cs + i * (2 * 8 * 16 * 512)) : ld16<AUX_SC1>(rW, to, ts); };
    gissue<9>(g0, ld);
    gwait<9>(g0, tag, ld);
    float M = asf(g0[8].x); M = fmaxf(M, dpp<XOR1>(M)); M = fmaxf(M, dpp<XOR2>(M)); M = fmaxf(M, dpp<XOR7>(M));
    float wg = ex2((asf(g0[8].x) - M) * LOG2E);
    float L = wg * asf(g0[8].z); L += dpp<XOR1>(L); L += dpp<XOR2>(L); L += dpp<XOR7>(L);
    wg /= L;
    f32x4 cb[2] = {Z4, Z4};
#pragma unroll
    for (int i = 0; i < 8; ++i) {
      const float wi = __shfl(wg, 2 * i + hf);
      cb[i >> 2] += wi * f32x4{bflo(g0[i].x), bfhi(g0[i].x), bflo(g0[i].z), bfhi(g0[i].z)};
    }
    LAS float* cl = lds + L_CW + w * 256;
    *(LAS f32x4*)(cl + hf * 128 + 4 * rl) = f32x4{tr32(cb[0][0], cb[1][0]), tr32(cb[0][1], cb[1][1]), tr32(cb[0][2], cb[1][2]), tr32(cb[0][3], cb[1][3])};
    LGKM0();
    float a2[16];
#pragma unroll
    for (int e = 0; e < 16; ++e) a2[e] = 0.f;
#pragma unroll
    for (int i = 0; i < 16; ++i) {
      const float c0 = cl[8 * i + (lane >> 3)], c1 = cl[128 + 8 * i + (lane >> 3)];
#pragma unroll
      for (int e = 0; e < 4; ++e) {
        const float lo = bflo(wv[i][e]), hi = bfhi(wv[i][e]);
        a2[2 * e] = fmaf(c0, lo, a2[2 * e]); a2[2 * e + 1] = fmaf(c0, hi, a2[2 * e + 1]);
        a2[8 + 2 * e] = fmaf(c1, lo, a2[8 + 2 * e]); a2[9 + 2 * e] = fmaf(c1, hi, a2[9 + 2 * e]);
      }
    }
    float a8[8], a4[4];
#pragma unroll
    for (int e = 0; e < 8; ++e) a8[e] = tr32(a2[e], a2[e + 8]);
#pragma unroll
    for (int e = 0; e < 4; ++e) a4[e] = tr16(a8[e], a8[e + 4]);
    *(LAS f32x2*)(lds + L_XR + w * 128 + hf * 64 + (lane & 7) * 8 + (g4 & 1) * 4 + ((lane >> 3) & 1) * 2) =
        f32x2{trd<XOR8>(a4[0], a4[2], lane & 8), trd<XOR8>(a4[1], a4[3], lane & 8)};
    gbar(ctl, phase, lane);
    f32x4 d[8];
#pragma unroll
    for (int ks = 0; ks < 2; ++ks) {
      f32x4 lo = Z4, hi = Z4;
#pragma unroll
      for (int ww = 0; ww < 4; ++ww) {
        const LAS float* sx = lds + L_XR + ww * 128 + ((l15 >> 1) & 1) * 64 + 32 * ks + 8 * g4;
        lo += *(const LAS f32x4*)sx; hi += *(const LAS f32x4*)(sx + 4);
      }
      v4u p = {cvtpk(lo[0], lo[1]), cvtpk(lo[2], lo[3]), cvtpk(hi[0], hi[1]), cvtpk(hi[2], hi[3])};
      if (l15 & 1) p = v4u{cvtpk(lo[0] - bflo(p[0]), lo[1] - bfhi(p[0])), cvtpk(lo[2] - bflo(p[1]), lo[3] - bfhi(p[1])),
                           cvtpk(hi[0] - bflo(p[2]), hi[1] - bfhi(p[2])), cvtpk(hi[2] - bflo(p[3]), hi[3] - bfhi(p[3]))};
#pragma unroll
      for (int i = 0; i < 8; ++i) d[i] = MFMA(p, bfr[8 * ks + i], ks ? d[i] : Z4);
    }
    yb0 = cvtpk(SEL4(d[0][0] + d[0][1], d[2][0] + d[2][1], d[4][0] + d[4][1], d[6][0] + d[6][1]),
                SEL4(d[1][0] + d[1][1], d[3][0] + d[3][1], d[5][0] + d[5][1], d[7][0] + d[7][1]));
    yb1 = cvtpk(SEL4(d[0][2] + d[0][3], d[2][2] + d[2][3], d[4][2] + d[4][3], d[6][2] + d[6][3]),
                SEL4(d[1][2] + d[1][3], d[3][2] + d[3][3], d[5][2] + d[5][3], d[7][2] + d[7][3]));
    SBAR();
  }
  lds_wait(ctl + 8 + w, 5);
#pragma unroll
  for (int ks = 0; ks < 4; ++ks) {
    v4u t[8];
#pragma unroll
    for (int i = 0; i < 8; ++i) t[i] = *(LAS v4u*)(lds + (ks < 2 ? L_T1 : L_T0) + ((w * 16 + 8 * (ks & 1) + i) * 64 + lane) * 4);
#pragma unroll
    for (int i = 0; i < 8; ++i) { bfr[8 * ks + i] = BFRAG(t, 0, i); asm volatile("" : "+v"(bfr[8 * ks + i])); }
  }
  const rsrc_t rOut = mkrs(out, 2 * 896 * 2);
#define FINAL_SUM(YBASE, COL0, NL, UN, WGT) { v4u g[NL]; const int ub = (UN) >= 14, ul = (UN) - 14 * ub; \
    const u32 yo = (8 * (NL) * w + (lane >> 3)) * 2048 + (lane & 7) * 16, ys = (YBASE) + ub * (32 * (NL)) * 2048 + ul * 128; \
    auto ld = [&](int i) { return ld16<AUX_SC1>(rW, yo, ys + i * 8 * 2048); }; \
    gissue<NL>(g, ld); gwait<NL, TPOLL>(g, tag, ld); \
    f32x4 sv = Z4; \
    _Pragma("unroll") for (int i = 0; i < (NL); ++i) sv += ((WGT) ? wrow[i & 3] : 1.f) * f32x4{bflo(g[i].x), bfhi(g[i].x), bflo(g[i].z), bfhi(g[i].z)}; \
    float v = tr16(tr32(sv[0], sv[2]), tr32(sv[1], sv[3])); v += dpp<XOR8>(v); \
    if (!(lane & 8)) lds[L_CW + w * 32 + 4 * (lane & 7) + 2 * hf + (g4 & 1)] = v; \
    gbar(ctl, phase, lane); \
    if (w == 0 && lane < 16) { f32x2 o = {0.f, 0.f}; \
      _Pragma("unroll") for (int ww = 0; ww < 4; ++ww) o += *(LAS f32x2*)(lds + L_CW + ww * 32 + 2 * lane); \
      __builtin_amdgcn_raw_buffer_store_b32(cvtpk(o[0], o[1]), rOut, (ub * 896 + (COL0) + 32 * ul + 2 * lane) * 2, 0, 0); } }
  float wrow[4] = {0.f, 0.f, 0.f, 0.f};
  if (id < 28) {
    v4u g[4];
    const u32 so = (32 * w + (lane >> 3)) * 128, ss = WS_YS + (id >= 14 ? 128 * 128 : 0);
    auto ld = [&](int i) { return ld16<AUX_SC1>(rW, so, ss + i * 8 * 128); };
    gissue<4>(g, ld); gwait<4>(g, tag, ld);
#pragma unroll
    for (int i = 0; i < 4; ++i) {
      float M = fmaxf(asf(g[i].x), dpp<XOR8>(asf(g[i].x))); M = mx16(M);
      const float wi = ex2((asf(g[i].x) - M) * LOG2E);
      float L = wi * asf(g[i].z); L += dpp<XOR8>(L); L = tr16(L, L);
      wrow[i] = wi / L;
    }
  }
  lds_wait(ctl + 4, 4);
  f32x4 d[8];
#pragma unroll
  for (int ks = 0; ks < 4; ++ks) {
    const v4u a = *(LAS v4u*)(lds + L_OB + (l15 & 7) * 64 + 16 * ks + 4 * g4);
#pragma unroll
    for (int i = 0; i < 8; ++i) d[i] = MFMA(a, bfr[8 * ks + i], ks ? d[i] : Z4);
  }
  float ds[8];
#pragma unroll
  for (int i = 0; i < 8; ++i) ds[i] = (d[i][0] + d[i][1]) + (d[i][2] + d[i][3]);
  const float y0 = tr16(hf ? ds[4] : ds[0], hf ? ds[6] : ds[2]), y1 = tr16(hf ? ds[5] : ds[1], hf ? ds[7] : ds[3]);
  if (w < 3 || l15 < 8) {
    st8(rW, WS_YA + ((vb * 128 + h * 8 + r8) * 256 + 64 * w + 4 * l15 + g4) * 8, cvtpk(y0, y1), tag);
    if (!isB) {
      const u32 yo2 = WS_YB + ((h * 8 + r8) * 256 + 64 * w + 4 * l15 + g4) * 8;
      st8(rW, yo2, yb0, tag);
      st8(rW, yo2 + 128 * 256 * 8, yb1, tag);
    }
  }
  if (id >= 28 && id < 56) FINAL_SUM(WS_YB, 448, 4, id - 28, false)
  if (id < 28) {
    FINAL_SUM(WS_YA, 0, 4, id, true)
    if (tid == 256 && id == 0) __builtin_amdgcn_raw_buffer_store_b32(tag, rW, WS_EPOCH, 0, AUX_SC1);
  }
}

extern "C" void kernel_launch(void* const* d_in, const int* in_sizes, int n_in, void* d_out, int out_size, void* d_ws,
                              size_t ws_size, hipStream_t stream) {
  fused_decode<<<256, 512, 0, stream>>>((const u16*)d_in[0], (const u16*)d_in[1], (const u16*)d_in[2], (const u16*)d_in[3],
                                        (const u16*)d_in[4], (const u16*)d_in[5], (const u16*)d_in[6], (const u16*)d_in[7],
                                        (const u16*)d_in[8], (const u16*)d_in[9], (const u16*)d_in[10], (u16*)d_out,
                                        (unsigned char*)d_ws);
}
```
